```python
import torch

def _softmax(x, axis=-1):
    return torch.softmax(x, dim=axis)
def _gelu(x):
    return torch.nn.functional.gelu(x, approximate="tanh")
def _silu(x):
    return torch.nn.functional.silu(x)
def _sigmoid(x):
    return torch.sigmoid(x)
def _layernorm(x, axis=-1, eps=1e-5):
    m = x.mean(dim=axis, keepdim=True)
    v = x.var(dim=axis, keepdim=True, unbiased=False)
    return (x - m) / torch.sqrt(v + eps)
def _scatter_add(tgt, idx, src, axis=0):
    return tgt.clone().index_add_(axis, idx, src)
def _segment_sum(src, seg, n):
    out = torch.zeros((n,) + src.shape[1:], dtype=src.dtype)
    return out.index_add_(0, seg, src)
def _pad(x, pw):
    flat = tuple(v for pair in reversed(pw) for v in pair)
    return torch.nn.functional.pad(x, flat)

kernel_name = 'standalone__sliding_block_cached__decode'

def setup_inputs():

    _w = (lambda a: (lambda t: t.to(torch.bfloat16) if t.is_floating_point() else t)(torch.as_tensor(a)))
    B, Sq, D = 1, 1, 640
    Hq, Hkv, Sk, Dh = 32, 32, 32768, 192
    assert Hq % Hkv == 0, (Hq, Hkv)
    return {
        "x": _w((lambda *s: torch.randn(*s, dtype=torch.bfloat16) * 0.8)(B, Sq, D)),
        "k_cache": _w((lambda *s: torch.randn(*s, dtype=torch.bfloat16) * 0.8)(B, Hkv, Sk, Dh)),
        "v_cache": _w((lambda *s: torch.randn(*s, dtype=torch.bfloat16) * 0.05)(B, Hkv, Sk, Dh)),
        "wq": _w((lambda *s: torch.randn(*s, dtype=torch.bfloat16) * 0.8)(D, Hq * Dh) * (D ** -0.5)),
        "wo": _w((lambda *s: torch.randn(*s, dtype=torch.bfloat16) * 0.05)(Hq * Dh, D) * ((Hq * Dh) ** -0.5)),
    }

def reference(x, k_cache, v_cache, wq, wo):
    B, Sq, D = x.shape
    Hq, Dh = 32, 192
    Hkv, Sk = k_cache.shape[1], k_cache.shape[2]
    W = min(4096, max(256, Sk // 4))
    rep = Hq // Hkv
    q = (lambda x, *d: x.permute(*d))((x @ wq).reshape(B, Sq, Hq, Dh), 0, 2, 1, 3)
    k = (lambda x, n, axis: torch.repeat_interleave(x, n, dim=axis))(k_cache[:, :, Sk - W:, :], rep, axis=1)
    v = (lambda x, n, axis: torch.repeat_interleave(x, n, dim=axis))(v_cache[:, :, Sk - W:, :], rep, axis=1)
    scores = torch.einsum("bhqd,bhkd->bhqk", q, k) * (Dh ** -0.5)
    p = _softmax(scores, axis=-1)
    o = torch.einsum("bhqk,bhkd->bhqd", p, v)
    return (lambda x, *d: x.permute(*d))(o, 0, 2, 1, 3).reshape(B, Sq, Hq * Dh) @ wo
```

```cpp
#include <hip/hip_runtime.h>
#include <hip/hip_bf16.h>
#include <stdint.h>

typedef unsigned v4u __attribute__((ext_vector_type(4)));
typedef __attribute__((ext_vector_type(2))) __bf16 bf16x2;

constexpr int D = 640, H = 32, SK = 32768, DH = 192, W = 4096, NS = 8, KPS = W / NS;

__device__ __forceinline__ float bflo(unsigned u) { return __builtin_bit_cast(float, u << 16); }
__device__ __forceinline__ float bfhi(unsigned u) { return __builtin_bit_cast(float, u & 0xffff0000u); }
__device__ __forceinline__ float bf2f(unsigned short u) { return __builtin_bit_cast(float, (unsigned)u << 16); }
__device__ __forceinline__ float rbf(float f) { return (float)(__bf16)f; }

__global__ void __launch_bounds__(256) k_q(const unsigned short* x, const unsigned short* wq, float* q) {
  __shared__ float red[32][64];
  const int tid = threadIdx.x, c = tid & 7, r = tid >> 3;
  const int col0 = blockIdx.x * 64 + c * 8;
  float acc[8] = {0, 0, 0, 0, 0, 0, 0, 0};
  for (int i = 0; i < 20; ++i) {
    const int row = r + 32 * i;
    v4u w = *(const v4u*)(wq + (size_t)row * (H * DH) + col0);
    float xv = bf2f(x[row]);
    for (int k = 0; k < 4; ++k) { acc[2 * k] += xv * bflo(w[k]); acc[2 * k + 1] += xv * bfhi(w[k]); }
  }
  for (int k = 0; k < 8; ++k) red[r][c * 8 + k] = acc[k];
  __syncthreads();
  if (tid < 64) {
    float s = 0;
    for (int i = 0; i < 32; ++i) s += red[i][tid];
    q[blockIdx.x * 64 + tid] = rbf(s);
  }
}

__global__ void __launch_bounds__(256) k_attn(const float* q, const unsigned short* kc, const unsigned short* vc, float* part) {
  __shared__ float sc[KPS];
  __shared__ float red[10][DH];
  __shared__ float wred[8];
  const int h = blockIdx.x >> 3, s = blockIdx.x & 7, tid = threadIdx.x;
  const size_t base = ((size_t)h * SK + (SK - W) + s * KPS) * DH;
  const int c = tid & 7, r = tid >> 3;
  float qv[24];
  for (int j = 0; j < 3; ++j) for (int k = 0; k < 8; ++k) qv[j * 8 + k] = q[h * DH + j * 64 + c * 8 + k];
  for (int i = 0; i < KPS / 32; ++i) {
    const int key = r + 32 * i;
    float a = 0;
    for (int j = 0; j < 3; ++j) {
      v4u w = *(const v4u*)(kc + base + (size_t)key * DH + j * 64 + c * 8);
      for (int k = 0; k < 4; ++k) { a += qv[j * 8 + 2 * k] * bflo(w[k]); a += qv[j * 8 + 2 * k + 1] * bfhi(w[k]); }
    }
    a += __shfl_xor(a, 1); a += __shfl_xor(a, 2); a += __shfl_xor(a, 4);
    if (c == 0) sc[key] = a * 0.07216878364870322f;
  }
  __syncthreads();
  float m = fmaxf(sc[tid], sc[tid + 256]);
  for (int o = 32; o; o >>= 1) m = fmaxf(m, __shfl_xor(m, o));
  if ((tid & 63) == 0) wred[tid >> 6] = m;
  __syncthreads();
  m = fmaxf(fmaxf(wred[0], wred[1]), fmaxf(wred[2], wred[3]));
  float p0 = __expf(sc[tid] - m), p1 = __expf(sc[tid + 256] - m);
  float l = p0 + p1;
  for (int o = 32; o; o >>= 1) l += __shfl_xor(l, o);
  if ((tid & 63) == 0) wred[4 + (tid >> 6)] = l;
  sc[tid] = p0; sc[tid + 256] = p1;
  __syncthreads();
  l = wred[4] + wred[5] + wred[6] + wred[7];
  const int c2 = tid % 24, r2 = tid / 24;
  float acc[8] = {0, 0, 0, 0, 0, 0, 0, 0};
  if (r2 < 10) {
    for (int key = r2; key < KPS; key += 10) {
      v4u w = *(const v4u*)(vc + base + (size_t)key * DH + c2 * 8);
      float p = sc[key];
      for (int k = 0; k < 4; ++k) { acc[2 * k] += p * bflo(w[k]); acc[2 * k + 1] += p * bfhi(w[k]); }
    }
    for (int k = 0; k < 8; ++k) red[r2][c2 * 8 + k] = acc[k];
  }
  __syncthreads();
  float* o = part + (size_t)blockIdx.x * 256;
  if (tid < DH) { float t = 0; for (int i = 0; i < 10; ++i) t += red[i][tid]; o[tid] = t; }
  if (tid == 0) { o[192] = m; o[193] = l; }
}

__global__ void __launch_bounds__(256) k_comb(const float* part, const unsigned short* wo, float* y) {
  __shared__ float o[DH];
  __shared__ float red[3][D];
  const int h = blockIdx.x, tid = threadIdx.x;
  const float* p = part + (size_t)h * 8 * 256;
  float M = -INFINITY;
  for (int s = 0; s < 8; ++s) M = fmaxf(M, p[s * 256 + 192]);
  float L = 0;
  for (int s = 0; s < 8; ++s) L += p[s * 256 + 193] * __expf(p[s * 256 + 192] - M);
  if (tid < DH) { float t = 0; for (int s = 0; s < 8; ++s) t += p[s * 256 + tid] * __expf(p[s * 256 + 192] - M); o[tid] = rbf(t / L); }
  __syncthreads();
  const int c = tid % 80, r = tid / 80;
  if (r < 3) {
    float acc[8] = {0, 0, 0, 0, 0, 0, 0, 0};
    for (int row = r; row < DH; row += 3) {
      v4u w = *(const v4u*)(wo + (size_t)(h * DH + row) * D + c * 8);
      float ov = o[row];
      for (int k = 0; k < 4; ++k) { acc[2 * k] += ov * bflo(w[k]); acc[2 * k + 1] += ov * bfhi(w[k]); }
    }
    for (int k = 0; k < 8; ++k) red[r][c * 8 + k] = acc[k];
  }
  __syncthreads();
  for (int i = tid; i < D; i += 256) y[h * D + i] = red[0][i] + red[1][i] + red[2][i];
}

__global__ void __launch_bounds__(64) k_fin(const float* y, __hip_bfloat16* out) {
  const int i = blockIdx.x * 64 + threadIdx.x;
  float t = 0;
  for (int h = 0; h < H; ++h) t += y[h * D + i];
  out[i] = __float2bfloat16(t);
}

extern "C" void kernel_launch(void* const* d_in, const int* in_sizes, int n_in, void* d_out, int out_size, void* d_ws, size_t ws_size, hipStream_t stream) {
  const unsigned short* x = (const unsigned short*)d_in[0];
  const unsigned short* kc = (const unsigned short*)d_in[1];
  const unsigned short* vc = (const unsigned short*)d_in[2];
  const unsigned short* wq = (const unsigned short*)d_in[3];
  const unsigned short* wo = (const unsigned short*)d_in[4];
  float* q = (float*)d_ws;
  float* part = q + 8192;
  float* y = part + 256 * 256;
  k_q<<<96, 256, 0, stream>>>(x, wq, q);
  k_attn<<<256, 256, 0, stream>>>(q, kc, vc, part);
  k_comb<<<32, 256, 0, stream>>>(part, wo, y);
  k_fin<<<10, 64, 0, stream>>>(y, (__hip_bfloat16*)d_out);
}
```

```cpp
#include <hip/hip_runtime.h>
#include <hip/hip_bf16.h>
#include <stdint.h>

typedef unsigned short u16;
typedef unsigned long long u64;
typedef unsigned v4u __attribute__((ext_vector_type(4)));
typedef unsigned v2u __attribute__((ext_vector_type(2)));
typedef float f4 __attribute__((ext_vector_type(4)));
typedef float f2 __attribute__((ext_vector_type(2)));
typedef __bf16 bf16x2 __attribute__((ext_vector_type(2)));
typedef __bf16 bf16x8 __attribute__((ext_vector_type(8)));
typedef __amdgpu_buffer_rsrc_t rsrc_t;

#define PRETOUCH 1
#define WO_GATE 16
#define XMAP 0

constexpr int D = 640, H = 32, SK = 32768, DH = 192, W = 4096, NS = 8, KPS = W / NS;
constexpr int NW = 16, KPW = KPS / NW, NG = KPW / 8;
constexpr int AUX_NT = 2, AUX_SC1 = 16;
constexpr int WS_EPOCH = 0;
constexpr int WS_GQ = 4096;
constexpr int GQ_STRIDE = 12 * 128;
constexpr int WS_G1 = WS_GQ + 256 * GQ_STRIDE;
constexpr int G1_STRIDE = 13 * 128;
constexpr int WS_G2 = WS_G1 + 256 * G1_STRIDE;
constexpr int G2_STRIDE = 21 * 128;
constexpr int WS_END = WS_G2 + 256 * G2_STRIDE;
constexpr int PSTR = 200;
constexpr unsigned SPIN_CAP = 1u << 16;

#define STAMP(i)

__device__ __forceinline__ rsrc_t mk_rsrc(const void* p, int bytes) { return __builtin_amdgcn_make_buffer_rsrc((void*)p, 0, bytes, 0x00020000); }
__device__ __forceinline__ float asf(unsigned u) { return __builtin_bit_cast(float, u); }
__device__ __forceinline__ unsigned asu(float f) { return __builtin_bit_cast(unsigned, f); }
template <int CTRL> __device__ __forceinline__ float dpp(float x) { return asf(__builtin_amdgcn_mov_dpp(asu(x), CTRL, 0xf, 0xf, true)); }
constexpr int XOR1 = 0xB1, XOR2 = 0x4E, HMIR = 0x141, ROR8 = 0x128, ROR4 = 0x124;
__device__ __forceinline__ unsigned pk_bf16(float lo, float hi) { bf16x2 r = {(__bf16)lo, (__bf16)hi}; return __builtin_bit_cast(unsigned, r); }
__device__ __forceinline__ float dot2(unsigned a, unsigned b, float c) { return __builtin_amdgcn_fdot2_f32_bf16(__builtin_bit_cast(bf16x2, a), __builtin_bit_cast(bf16x2, b), c, false); }
__device__ __forceinline__ f4 mfma(v4u a, v4u b, f4 c) { return __builtin_amdgcn_mfma_f32_16x16x32_bf16(__builtin_bit_cast(bf16x8, a), __builtin_bit_cast(bf16x8, b), c, 0, 0, 0); }
__device__ __forceinline__ float sum8(float x) { x += dpp<XOR1>(x); x += dpp<XOR2>(x); x += dpp<HMIR>(x); return x; }
__device__ __forceinline__ float max8(float x) { x = fmaxf(x, dpp<XOR1>(x)); x = fmaxf(x, dpp<XOR2>(x)); x = fmaxf(x, dpp<HMIR>(x)); return x; }
__device__ __forceinline__ void swap32(float& a, float& b) { auto r = __builtin_amdgcn_permlane32_swap(asu(a), asu(b), false, false); a = asf(r[0]); b = asf(r[1]); }
__device__ __forceinline__ void swap16(float& a, float& b) { auto r = __builtin_amdgcn_permlane16_swap(asu(a), asu(b), false, false); a = asf(r[0]); b = asf(r[1]); }
__device__ __forceinline__ float sum_hi(float x) { x += dpp<ROR8>(x); float y = x; swap16(x, y); x += y; y = x; swap32(x, y); return x + y; }
__device__ __forceinline__ float max_hi(float x) { x = fmaxf(x, dpp<ROR8>(x)); float y = x; swap16(x, y); x = fmaxf(x, y); y = x; swap32(x, y); return fmaxf(x, y); }
#define BARRIER() do { __builtin_amdgcn_sched_barrier(0); asm volatile("s_waitcnt lgkmcnt(0)\n\ts_barrier" ::: "memory"); __builtin_amdgcn_sched_barrier(0); } while (0)
__device__ __forceinline__ bool tagged(v4u g, unsigned tag) { return (g[1] == tag) & (g[3] == tag); }

__device__ __forceinline__ void fold_tile(f4 d, float& o0, float& o1) {
  float a = d[0], b = d[2], c = d[1], e = d[3];
  swap32(a, b); swap32(c, e);
  o0 = a + dpp<ROR8>(b); o1 = c + dpp<ROR8>(e);
}

__global__ void __launch_bounds__(NW * 64) fused_decode(const u16* __restrict__ x, const u16* __restrict__ kc, const u16* __restrict__ vc,
                                                        const u16* __restrict__ wq, const u16* __restrict__ wo, u16* __restrict__ out, unsigned char* ws
) {
  __shared__ __attribute__((aligned(16))) float lds[32 * PSTR + 96 + 64 + 16 + 16 + 4];
  float* P = lds;
  unsigned* qs = (unsigned*)(lds + 32 * PSTR);
  unsigned* os = qs + 96;
  float* ms = lds + 32 * PSTR + 96 + 64;
  float* ls = ms + 16;
  unsigned* ctl = (unsigned*)(ls + 16);
  STAMP(0);
  asm volatile("" :: "s"(x), "s"(kc), "s"(vc), "s"(wq), "s"(wo), "s"(out), "s"(ws));
  const int b = blockIdx.x, jb = b >> 3;
  const int h = XMAP ? jb : (b & 7) * 4 + (jb >> 3), s = XMAP ? b & 7 : jb & 7;
  const int tid = threadIdx.x, wave = __builtin_amdgcn_readfirstlane(tid >> 6), lane = tid & 63, r8 = lane >> 3, c7 = lane & 7;
  const rsrc_t rws = mk_rsrc(ws, WS_END);
  v4u msk;
#pragma unroll
  for (int d = 0; d < 4; ++d) msk[d] = ((c7 >> 1) == d) ? ((c7 & 1) ? 0xffff0000u : 0x0000ffffu) : 0u;
  unsigned tag;

  if (wave < 3) {
    const rsrc_t rx = mk_rsrc(x, D * 2);
    const rsrc_t rq = mk_rsrc(wq + (size_t)(80 * s) * (H * DH) + h * DH + wave * 64, 80 * H * DH * 2);
    const int xo = 160 * s + 20 * r8;
    const v4u xa = __builtin_amdgcn_raw_buffer_load_b128(rx, xo, 0, 0);
    const unsigned xb = __builtin_amdgcn_raw_buffer_load_b32(rx, xo + 16, 0, 0);
    v4u wr[10];
    const int wo_ = r8 * 10 * (H * DH * 2) + c7 * 16;
#pragma unroll
    for (int j = 0; j < 10; ++j) wr[j] = __builtin_amdgcn_raw_buffer_load_b128(rq, wo_, j * (H * DH * 2), AUX_NT);
    const unsigned ep = __builtin_amdgcn_raw_buffer_load_b32(rws, 0, WS_EPOCH, AUX_SC1);
    if (tid < 2) ctl[tid] = 0;
    BARRIER();
    STAMP(1);
    f4 acc = {0.f, 0.f, 0.f, 0.f};
#pragma unroll
    for (int j = 0; j < 10; ++j) {
      const unsigned xw = j < 8 ? xa[j >> 1] : xb;
      const unsigned xx = __builtin_amdgcn_perm(xw, xw, (j & 1) ? 0x03020302u : 0x01000100u);
      acc = mfma(msk & xx, wr[j], acc);
    }
    tag = __builtin_amdgcn_readfirstlane(ep) * 0x9E3779B1u + 0x7F4A7C15u;
    float q0, q1; fold_tile(acc, q0, q1);
    STAMP(2);
    if ((lane & 8) == 0)
      __builtin_amdgcn_raw_buffer_store_b128(v4u{asu(q0), tag, asu(q1), tag}, rws, (lane >> 4) * 128 + c7 * 16, WS_GQ + (h * 8 + s) * GQ_STRIDE + wave * 512, AUX_SC1);
    __builtin_amdgcn_sched_barrier(0);
  } else {
    BARRIER();
    STAMP(1);
  }

  const size_t kvbase = ((size_t)h * SK + (SK - W) + s * KPS + wave * KPW) * DH;
  const rsrc_t rk = mk_rsrc(kc + kvbase, KPW * DH * 2), rv = mk_rsrc(vc + kvbase, KPW * DH * 2);
  const int lo = r8 * (DH * 2) + c7 * 16;
  v4u kr[NG * 3], vr[NG * 3];
#pragma unroll
  for (int i = 0; i < NG * 3; ++i) kr[i] = __builtin_amdgcn_raw_buffer_load_b128(rk, lo + (i % 3) * 128, (i / 3) * 8 * DH * 2, AUX_NT);
#pragma unroll
  for (int i = 0; i < NG * 3; ++i) vr[i] = __builtin_amdgcn_raw_buffer_load_b128(rv, lo + (i % 3) * 128, (i / 3) * 8 * DH * 2, AUX_NT);
  __builtin_amdgcn_sched_barrier(0);
  if (lane == 0) __hip_atomic_fetch_add(&ctl[1], 1u, __ATOMIC_RELAXED, __HIP_MEMORY_SCOPE_WORKGROUP);
  STAMP(4);
  if (wave < 3) {
    const int go = (4 * (lane >> 5)) * GQ_STRIDE + (lane & 31) * 16;
    const int gs = WS_GQ + h * 8 * GQ_STRIDE + wave * 512;
    v4u g[4];
    _Pragma("nounroll") for (unsigned spin = 0; spin < SPIN_CAP; ++spin) {
      bool ok = true;
#pragma unroll
      for (int i = 0; i < 4; ++i) g[i] = __builtin_amdgcn_raw_buffer_load_b128(rws, go, gs + i * GQ_STRIDE, AUX_SC1);
#pragma unroll
      for (int i = 0; i < 4; ++i) ok &= tagged(g[i], tag);
      if (__all(ok)) break;
      asm volatile("" ::: "memory");
    }
    float a0 = (asf(g[0][0]) + asf(g[1][0])) + (asf(g[2][0]) + asf(g[3][0]));
    float a1 = (asf(g[0][2]) + asf(g[1][2])) + (asf(g[2][2]) + asf(g[3][2]));
    float b0 = a0, b1 = a1;
    swap32(a0, b0); swap32(a1, b1);
    if (lane < 32) qs[32 * wave + 4 * c7 + 2 * (r8 & 1) + (r8 >> 1)] = pk_bf16(a0 + b0, a1 + b1);
    if (tid == 0) ctl[2] = tag;
    asm volatile("s_waitcnt lgkmcnt(0)" ::: "memory");
    if (lane == 0) __hip_atomic_fetch_add(&ctl[0], 1u, __ATOMIC_RELAXED, __HIP_MEMORY_SCOPE_WORKGROUP);
    STAMP(3);
  }
  while (__hip_atomic_load(&ctl[0], __ATOMIC_RELAXED, __HIP_MEMORY_SCOPE_WORKGROUP) != 3u) __builtin_amdgcn_s_sleep(2);
  asm volatile("" ::: "memory");
  v4u qv[3];
#pragma unroll
  for (int t = 0; t < 3; ++t) qv[t] = *(const v4u*)(qs + 32 * t + 4 * c7);
  tag = __builtin_amdgcn_readfirstlane(ctl[2]);

  constexpr float C2 = 0.07216878364870322f * 1.4426950408889634f;
  float sc[NG];
#pragma unroll
  for (int j = 0; j < NG; ++j) {
    float a = 0.f;
#pragma unroll
    for (int t = 0; t < 3; ++t)
#pragma unroll
      for (int k = 0; k < 4; ++k) a = dot2(kr[3 * j + t][k], qv[t][k], a);
    sc[j] = sum8(a) * C2;
  }
  float m = sc[0];
#pragma unroll
  for (int j = 1; j < NG; ++j) m = fmaxf(m, sc[j]);
  m = max_hi(m);
  STAMP(5);
  {
    f4 acc[3] = {{0.f, 0.f, 0.f, 0.f}, {0.f, 0.f, 0.f, 0.f}, {0.f, 0.f, 0.f, 0.f}};
    float l = 0.f;
    v4u pa[NG];
#pragma unroll
    for (int j = 0; j < NG; ++j) {
      const float p = __builtin_amdgcn_exp2f(sc[j] - m);
      l += p;
      pa[j] = msk & pk_bf16(p, p);
    }
    l = sum_hi(l);
    if (lane == 0) { ms[wave] = m; ls[wave] = l; }
    __builtin_amdgcn_sched_barrier(0);
    STAMP(6);
#pragma unroll
    for (int j = 0; j < NG; ++j)
#pragma unroll
      for (int t = 0; t < 3; ++t) acc[t] = mfma(pa[j], vr[3 * j + t], acc[t]);
    if ((lane >> 5) == ((lane >> 3) & 1)) {
      float* pp = P + (2 * wave + (lane >> 5)) * PSTR + 8 * c7 + 4 * ((lane >> 4) & 1);
#pragma unroll
      for (int t = 0; t < 3; ++t) *(f4*)(pp + 64 * t) = acc[t];
    }
  }
  STAMP(7);
  const int rq_ = s & 3, ch = s >> 2;
  if (wave >= 3 && wave < 8) {
    const int lc = (b & 7) * 5 + (jb >> 2), qd = jb & 3;
    const int fo = (lane >> 1) * G2_STRIDE + (lane & 1) * 16;
    const int fs = WS_G2 + (lc / 20) * 128 * G2_STRIDE + (lc % 20) * 128 + qd * 32;
    v4u g[4];
    if (wave == 3 && jb < 20) {
#pragma unroll
      for (int i = 0; i < 4; ++i) g[i] = __builtin_amdgcn_raw_buffer_load_b128(rws, fo, fs + i * 32 * G2_STRIDE, AUX_SC1);
      asm volatile("" :: "v"(g[0]), "v"(g[1]), "v"(g[2]), "v"(g[3]));
    }
    while (__hip_atomic_load(&ctl[1], __ATOMIC_RELAXED, __HIP_MEMORY_SCOPE_WORKGROUP) < WO_GATE) __builtin_amdgcn_s_sleep(2);
    asm volatile("" ::: "memory");
    const rsrc_t ro = mk_rsrc(wo + (size_t)(h * DH) * D + 320 * ch + 64 * (wave - 3), DH * D * 2);
    v4u wr[6];
#pragma unroll
    for (int j = 0; j < 6; ++j) {
      const int n = 3 * rq_ + (j >> 1);
      wr[j] = __builtin_amdgcn_raw_buffer_load_b128(ro, r8 * (4 * D * 2) + c7 * 16, (32 * (n >> 1) + 2 * (n & 1) + (j & 1)) * D * 2, AUX_NT);
    }
    STAMP(12);
    BARRIER();
    STAMP(8);
    BARRIER();
    BARRIER();
    STAMP(10);
    const v4u oa = *(const v4u*)(os + 8 * r8);
    const v2u ob = *(const v2u*)(os + 8 * r8 + 4);
    f4 y = {0.f, 0.f, 0.f, 0.f};
#pragma unroll
    for (int j = 0; j < 6; ++j) y = mfma(msk & (j < 4 ? oa[j] : ob[j - 4]), wr[j], y);
    float y0, y1; fold_tile(y, y0, y1);
    if ((lane & 8) == 0)
      __builtin_amdgcn_raw_buffer_store_b128(v4u{asu(y0), tag, asu(y1), tag}, rws, (lane >> 4) * 128 + c7 * 16, WS_G2 + ((ch * 32 + h) * 4 + rq_) * G2_STRIDE + (wave - 3) * 512, AUX_SC1);
    STAMP(13);
    if (wave == 3 && jb < 20) {
      bool ok[4] = {false, false, false, false};
      _Pragma("nounroll") for (unsigned spin = 0; spin < SPIN_CAP; ++spin) {
#pragma unroll
        for (int i = 0; i < 4; ++i) if (!ok[i]) g[i] = __builtin_amdgcn_raw_buffer_load_b128(rws, fo, fs + i * 32 * G2_STRIDE, AUX_SC1);
#pragma unroll
        for (int i = 0; i < 4; ++i) ok[i] = tagged(g[i], tag);
        if (__all(ok[0] & ok[1] & ok[2] & ok[3])) break;
        asm volatile("" ::: "memory");
      }
      STAMP(14);
      float a0 = (asf(g[0][0]) + asf(g[1][0])) + (asf(g[2][0]) + asf(g[3][0]));
      float a1 = (asf(g[0][2]) + asf(g[1][2])) + (asf(g[2][2]) + asf(g[3][2]));
      swap32(a0, a1); a0 += a1;
      { float a2 = a0; swap16(a0, a2); a0 += a2; }
      a0 += dpp<ROR8>(a0); a0 += dpp<ROR4>(a0); a0 += dpp<XOR2>(a0);
      if ((lane & 30) == 0) out[64 * (lc >> 2) + 8 * (2 * qd + (lane & 1)) + 4 * (lc & 1) + (lc & 2) + (lane >> 5)] = (u16)pk_bf16(a0, a0);
      if (b == 0 && lane == 0) __builtin_amdgcn_raw_buffer_store_b32(tag, rws, 0, WS_EPOCH, AUX_SC1);
    }
  } else if (wave == 0) {
    const int go = r8 * G1_STRIDE + c7 * 16, gs = WS_G1 + h * 8 * G1_STRIDE;
    v4u g[4];
#pragma unroll
    for (int i = 0; i < 3; ++i) g[i] = __builtin_amdgcn_raw_buffer_load_b128(rws, go + i * 128, gs + 3 * rq_ * 128, AUX_SC1);
    g[3] = __builtin_amdgcn_raw_buffer_load_b128(rws, go + 12 * 128, gs, AUX_SC1);
    asm volatile("" :: "v"(g[0]), "v"(g[1]), "v"(g[2]), "v"(g[3]));
    BARRIER();
    STAMP(8);
    BARRIER();
    STAMP(9);
    bool ok = false;
    _Pragma("nounroll") for (unsigned spin = 0; spin < SPIN_CAP; ++spin) {
      if (!ok) {
#pragma unroll
        for (int i = 0; i < 3; ++i) g[i] = __builtin_amdgcn_raw_buffer_load_b128(rws, go + i * 128, gs + 3 * rq_ * 128, AUX_SC1);
        g[3] = __builtin_amdgcn_raw_buffer_load_b128(rws, go + 12 * 128, gs, AUX_SC1);
        ok = tagged(g[0], tag) & tagged(g[1], tag) & tagged(g[2], tag) & tagged(g[3], tag);
      }
      if (__all(ok)) break;
      asm volatile("" ::: "memory");
    }
    STAMP(10);
    const float Ms = asf(g[3][0]), Mg = max_hi(Ms);
    const float wt = __builtin_amdgcn_exp2f(Ms - Mg);
    const float inv = 1.f / sum_hi(wt * asf(g[3][2]));
    float v0 = wt * asf(g[0][0]), v1 = wt * asf(g[0][2]), v2 = wt * asf(g[1][0]), v3 = wt * asf(g[1][2]), v4 = wt * asf(g[2][0]), v5 = wt * asf(g[2][2]);
    swap32(v0, v2); v0 += v2;
    swap32(v1, v3); v1 += v3;
    swap32(v4, v5); v4 += v5;
    swap16(v0, v1); v0 += v1;
    v5 = v4; swap16(v4, v5); v4 += v5;
    v0 += dpp<ROR8>(v0); v4 += dpp<ROR8>(v4);
    v0 *= inv; v4 *= inv;
    if ((lane & 8) == 0) { os[8 * c7 + (lane >> 4)] = pk_bf16(v0, v0); os[8 * c7 + 4 + (lane >> 5)] = pk_bf16(v4, v4); }
    BARRIER();
  } else {
    BARRIER();
    STAMP(8);
    const int g1 = WS_G1 + (h * 8 + s) * G1_STRIDE;
    if (wave >= 8 && wave < 14) {
      const int w = wave - 8;
      const float* pp = P + 4 * r8 * PSTR + 32 * w + 4 * c7;
      const f2 mw = *(const f2*)(ms + 2 * r8);
      const f4 u0 = *(const f4*)pp + *(const f4*)(pp + PSTR), u1 = *(const f4*)(pp + 2 * PSTR) + *(const f4*)(pp + 3 * PSTR);
      const float M = max_hi(fmaxf(mw[0], mw[1]));
      const f4 u = u0 * __builtin_amdgcn_exp2f(mw[0] - M) + u1 * __builtin_amdgcn_exp2f(mw[1] - M);
      float a = u[0], b2 = u[1], c = u[2], e = u[3];
      swap32(a, c); a += c;
      swap32(b2, e); b2 += e;
      c = a; swap16(a, c); a += c;
      e = b2; swap16(b2, e); b2 += e;
      a += dpp<ROR8>(a); b2 += dpp<ROR8>(b2);
      if ((lane & 24) == 0) __builtin_amdgcn_raw_buffer_store_b128(v4u{asu(a), tag, asu(b2), tag}, rws, (lane >> 5) * 128 + c7 * 16, g1 + w * 256, AUX_SC1);
    } else if (wave == 14) {
      const float mw = ms[lane & 15];
      float M = max8(mw); M = fmaxf(M, dpp<ROR8>(M));
      float L = sum8(ls[lane & 15] * __builtin_amdgcn_exp2f(mw - M)); L += dpp<ROR8>(L);
      if (lane < 8) __builtin_amdgcn_raw_buffer_store_b128(v4u{asu(M), tag, asu(L), tag}, rws, lane * 16, g1 + 12 * 128, AUX_SC1);
    }
    STAMP(9);
    BARRIER();
  }
  STAMP(11);
}

extern "C" void kernel_launch(void* const* d_in, const int* in_sizes, int n_in, void* d_out, int out_size, void* d_ws, size_t ws_size, hipStream_t stream) {
  fused_decode<<<256, NW * 64, 0, stream>>>((const u16*)d_in[0], (const u16*)d_in[1], (const u16*)d_in[2], (const u16*)d_in[3], (const u16*)d_in[4], (u16*)d_out, (unsigned char*)d_ws
  );
}
```

```cpp
#include <hip/hip_runtime.h>
#include <hip/hip_bf16.h>
#include <stdint.h>

typedef unsigned short u16;
typedef unsigned long long u64;
typedef unsigned v4u __attribute__((ext_vector_type(4)));
typedef unsigned v2u __attribute__((ext_vector_type(2)));
typedef float f4 __attribute__((ext_vector_type(4)));
typedef float f2 __attribute__((ext_vector_type(2)));
typedef __bf16 bf16x2 __attribute__((ext_vector_type(2)));
typedef __bf16 bf16x8 __attribute__((ext_vector_type(8)));
typedef __amdgpu_buffer_rsrc_t rsrc_t;

#define PRETOUCH 0
#define WO_GATE 16
#define XMAP 0

constexpr int D = 640, H = 32, SK = 32768, DH = 192, W = 4096, NS = 8, KPS = W / NS;
constexpr int NW = 16, KPW = KPS / NW, NG = KPW / 8;
constexpr int AUX_NT = 2, AUX_SC1 = 16;
constexpr int WS_EPOCH = 0;
constexpr int WS_GQ = 4096;
constexpr int GQ_STRIDE = 12 * 128;
constexpr int WS_G1 = WS_GQ + 256 * GQ_STRIDE;
constexpr int G1_STRIDE = 13 * 128;
constexpr int WS_G2 = WS_G1 + 256 * G1_STRIDE;
constexpr int G2_STRIDE = 21 * 128;
constexpr int WS_END = WS_G2 + 256 * G2_STRIDE;
constexpr int PSTR = 200;
constexpr unsigned SPIN_CAP = 1u << 16;

#define STAMP(i) do { if ((i) == 0 || (i) == 1 || (i) == 0) { u64 t_; __builtin_amdgcn_sched_barrier(0); asm volatile("s_memrealtime %0\n\ts_waitcnt lgkmcnt(0)" : "=s"(t_) :: "memory"); __builtin_amdgcn_sched_barrier(0); stv[i] = t_; } } while (0)
#define DIAG 1

__device__ __forceinline__ rsrc_t mk_rsrc(const void* p, int bytes) { return __builtin_amdgcn_make_buffer_rsrc((void*)p, 0, bytes, 0x00020000); }
__device__ __forceinline__ float asf(unsigned u) { return __builtin_bit_cast(float, u); }
__device__ __forceinline__ unsigned asu(float f) { return __builtin_bit_cast(unsigned, f); }
template <int CTRL> __device__ __forceinline__ float dpp(float x) { return asf(__builtin_amdgcn_mov_dpp(asu(x), CTRL, 0xf, 0xf, true)); }
constexpr int XOR1 = 0xB1, XOR2 = 0x4E, HMIR = 0x141, ROR8 = 0x128, ROR4 = 0x124;
__device__ __forceinline__ unsigned pk_bf16(float lo, float hi) { bf16x2 r = {(__bf16)lo, (__bf16)hi}; return __builtin_bit_cast(unsigned, r); }
__device__ __forceinline__ float dot2(unsigned a, unsigned b, float c) { return __builtin_amdgcn_fdot2_f32_bf16(__builtin_bit_cast(bf16x2, a), __builtin_bit_cast(bf16x2, b), c, false); }
__device__ __forceinline__ f4 mfma(v4u a, v4u b, f4 c) { return __builtin_amdgcn_mfma_f32_16x16x32_bf16(__builtin_bit_cast(bf16x8, a), __builtin_bit_cast(bf16x8, b), c, 0, 0, 0); }
__device__ __forceinline__ float sum8(float x) { x += dpp<XOR1>(x); x += dpp<XOR2>(x); x += dpp<HMIR>(x); return x; }
__device__ __forceinline__ float max8(float x) { x = fmaxf(x, dpp<XOR1>(x)); x = fmaxf(x, dpp<XOR2>(x)); x = fmaxf(x, dpp<HMIR>(x)); return x; }
__device__ __forceinline__ void swap32(float& a, float& b) { auto r = __builtin_amdgcn_permlane32_swap(asu(a), asu(b), false, false); a = asf(r[0]); b = asf(r[1]); }
__device__ __forceinline__ void swap16(float& a, float& b) { auto r = __builtin_amdgcn_permlane16_swap(asu(a), asu(b), false, false); a = asf(r[0]); b = asf(r[1]); }
__device__ __forceinline__ float sum_hi(float x) { x += dpp<ROR8>(x); float y = x; swap16(x, y); x += y; y = x; swap32(x, y); return x + y; }
__device__ __forceinline__ float max_hi(float x) { x = fmaxf(x, dpp<ROR8>(x)); float y = x; swap16(x, y); x = fmaxf(x, y); y = x; swap32(x, y); return fmaxf(x, y); }
#define BARRIER() do { __builtin_amdgcn_sched_barrier(0); asm volatile("s_waitcnt lgkmcnt(0)\n\ts_barrier" ::: "memory"); __builtin_amdgcn_sched_barrier(0); } while (0)
__device__ __forceinline__ bool tagged(v4u g, unsigned tag) { return (g[1] == tag) & (g[3] == tag); }

__device__ __forceinline__ void fold_tile(f4 d, float& o0, float& o1) {
  float a = d[0], b = d[2], c = d[1], e = d[3];
  swap32(a, b); swap32(c, e);
  o0 = a + dpp<ROR8>(b); o1 = c + dpp<ROR8>(e);
}

__global__ void __launch_bounds__(NW * 64) fused_decode(const u16* x_, const u16* kc_, const u16* vc_, const u16* wq_, const u16* wo_, u16* out_, unsigned char* ws_
) {
  __shared__ __attribute__((aligned(16))) float lds[32 * PSTR + 96 + 64 + 16 + 16 + 4];
  float* P = lds;
  unsigned* qs = (unsigned*)(lds + 32 * PSTR);
  unsigned* os = qs + 96;
  float* ms = lds + 32 * PSTR + 96 + 64;
  float* ls = ms + 16;
  unsigned* ctl = (unsigned*)(ls + 16);
  u64 stv[16] = {};
  STAMP(0);
  typedef u64 u64x4 __attribute__((ext_vector_type(4)));
  typedef u64 u64x2 __attribute__((ext_vector_type(2)));
  u64x4 ka; u64x2 kb; u64 kd;
  asm volatile("s_load_dwordx8 %0, %3, 0x0\n\ts_load_dwordx4 %1, %3, 0x20\n\ts_load_dwordx2 %2, %3, 0x30\n\ts_waitcnt lgkmcnt(0)"
               : "=&s"(ka), "=&s"(kb), "=&s"(kd) : "s"(__builtin_amdgcn_kernarg_segment_ptr()));
  const u16 *x = (const u16*)ka[0], *kc = (const u16*)ka[1], *vc = (const u16*)ka[2], *wq = (const u16*)ka[3], *wo = (const u16*)kb[0];
  __attribute__((address_space(1))) u16* out = (__attribute__((address_space(1))) u16*)kb[1];
  unsigned char* ws = (unsigned char*)kd;
  const int b = blockIdx.x, jb = b >> 3;
  const int h = XMAP ? jb : (b & 7) * 4 + (jb >> 3), s = XMAP ? b & 7 : jb & 7;
  const int tid = threadIdx.x, wave = __builtin_amdgcn_readfirstlane(tid >> 6), lane = tid & 63, r8 = lane >> 3, c7 = lane & 7;
  const rsrc_t rws = mk_rsrc(ws, WS_END);
  v4u msk;
#pragma unroll
  for (int d = 0; d < 4; ++d) msk[d] = ((c7 >> 1) == d) ? ((c7 & 1) ? 0xffff0000u : 0x0000ffffu) : 0u;
  unsigned tag;

  if (wave < 3) {
    const rsrc_t rx = mk_rsrc(x, D * 2);
    const rsrc_t rq = mk_rsrc(wq + (size_t)(80 * s) * (H * DH) + h * DH + wave * 64, 80 * H * DH * 2);
    const int xo = 160 * s + 20 * r8;
    const v4u xa = __builtin_amdgcn_raw_buffer_load_b128(rx, xo, 0, 0);
    const unsigned xb = __builtin_amdgcn_raw_buffer_load_b32(rx, xo + 16, 0, 0);
    v4u wr[10];
    const int wo_ = r8 * 10 * (H * DH * 2) + c7 * 16;
#pragma unroll
    for (int j = 0; j < 10; ++j) wr[j] = __builtin_amdgcn_raw_buffer_load_b128(rq, wo_, j * (H * DH * 2), AUX_NT);
    const unsigned ep = __builtin_amdgcn_raw_buffer_load_b32(rws, 0, WS_EPOCH, AUX_SC1);
    if (tid < 2) ctl[tid] = 0;
    BARRIER();
    STAMP(1);
    f4 acc = {0.f, 0.f, 0.f, 0.f};
#pragma unroll
    for (int j = 0; j < 10; ++j) {
      const unsigned xw = j < 8 ? xa[j >> 1] : xb;
      const unsigned xx = __builtin_amdgcn_perm(xw, xw, (j & 1) ? 0x03020302u : 0x01000100u);
      acc = mfma(msk & xx, wr[j], acc);
    }
    tag = __builtin_amdgcn_readfirstlane(ep) * 0x9E3779B1u + 0x7F4A7C15u;
    float q0, q1; fold_tile(acc, q0, q1);
    STAMP(2);
    if ((lane & 8) == 0)
      __builtin_amdgcn_raw_buffer_store_b128(v4u{asu(q0), tag, asu(q1), tag}, rws, (lane >> 4) * 128 + c7 * 16, WS_GQ + (h * 8 + s) * GQ_STRIDE + wave * 512, AUX_SC1);
    __builtin_amdgcn_sched_barrier(0);
  } else {
    BARRIER();
    STAMP(1);
  }

  const size_t kvbase = ((size_t)h * SK + (SK - W) + s * KPS + wave * KPW) * DH;
  const rsrc_t rk = mk_rsrc(kc + kvbase, KPW * DH * 2), rv = mk_rsrc(vc + kvbase, KPW * DH * 2);
  const int lo = r8 * (DH * 2) + c7 * 16;
  v4u kr[NG * 3], vr[NG * 3];
#pragma unroll
  for (int i = 0; i < NG * 3; ++i) kr[i] = __builtin_amdgcn_raw_buffer_load_b128(rk, lo + (i % 3) * 128, (i / 3) * 8 * DH * 2, AUX_NT);
#pragma unroll
  for (int i = 0; i < NG * 3; ++i) vr[i] = __builtin_amdgcn_raw_buffer_load_b128(rv, lo + (i % 3) * 128, (i / 3) * 8 * DH * 2, AUX_NT);
  __builtin_amdgcn_sched_barrier(0);
  if (lane == 0) __hip_atomic_fetch_add(&ctl[1], 1u, __ATOMIC_RELAXED, __HIP_MEMORY_SCOPE_WORKGROUP);
  STAMP(4);
  if (wave < 3) {
    const int go = (4 * (lane >> 5)) * GQ_STRIDE + (lane & 31) * 16;
    const int gs = WS_GQ + h * 8 * GQ_STRIDE + wave * 512;
    v4u g[4];
    _Pragma("nounroll") for (unsigned spin = 0; spin < SPIN_CAP; ++spin) {
      bool ok = true;
#pragma unroll
      for (int i = 0; i < 4; ++i) g[i] = __builtin_amdgcn_raw_buffer_load_b128(rws, go, gs + i * GQ_STRIDE, AUX_SC1);
#pragma unroll
      for (int i = 0; i < 4; ++i) ok &= tagged(g[i], tag);
      if (__all(ok)) break;
      asm volatile("" ::: "memory");
    }
    float a0 = (asf(g[0][0]) + asf(g[1][0])) + (asf(g[2][0]) + asf(g[3][0]));
    float a1 = (asf(g[0][2]) + asf(g[1][2])) + (asf(g[2][2]) + asf(g[3][2]));
    float b0 = a0, b1 = a1;
    swap32(a0, b0); swap32(a1, b1);
    if (lane < 32) qs[32 * wave + 4 * c7 + 2 * (r8 & 1) + (r8 >> 1)] = pk_bf16(a0 + b0, a1 + b1);
    if (tid == 0) ctl[2] = tag;
    asm volatile("s_waitcnt lgkmcnt(0)" ::: "memory");
    if (lane == 0) __hip_atomic_fetch_add(&ctl[0], 1u, __ATOMIC_RELAXED, __HIP_MEMORY_SCOPE_WORKGROUP);
    STAMP(3);
  }
  while (__hip_atomic_load(&ctl[0], __ATOMIC_RELAXED, __HIP_MEMORY_SCOPE_WORKGROUP) != 3u) __builtin_amdgcn_s_sleep(2);
  asm volatile("" ::: "memory");
  v4u qv[3];
#pragma unroll
  for (int t = 0; t < 3; ++t) qv[t] = *(const v4u*)(qs + 32 * t + 4 * c7);
  tag = __builtin_amdgcn_readfirstlane(ctl[2]);

  constexpr float C2 = 0.07216878364870322f * 1.4426950408889634f;
  float sc[NG];
#pragma unroll
  for (int j = 0; j < NG; ++j) {
    float a = 0.f;
#pragma unroll
    for (int t = 0; t < 3; ++t)
#pragma unroll
      for (int k = 0; k < 4; ++k) a = dot2(kr[3 * j + t][k], qv[t][k], a);
    sc[j] = sum8(a) * C2;
  }
  float m = sc[0];
#pragma unroll
  for (int j = 1; j < NG; ++j) m = fmaxf(m, sc[j]);
  m = max_hi(m);
  STAMP(5);
  {
    f4 acc[3] = {{0.f, 0.f, 0.f, 0.f}, {0.f, 0.f, 0.f, 0.f}, {0.f, 0.f, 0.f, 0.f}};
    float l = 0.f;
    v4u pa[NG];
#pragma unroll
    for (int j = 0; j < NG; ++j) {
      const float p = __builtin_amdgcn_exp2f(sc[j] - m);
      l += p;
      pa[j] = msk & pk_bf16(p, p);
    }
    l = sum_hi(l);
    if (lane == 0) { ms[wave] = m; ls[wave] = l; }
    __builtin_amdgcn_sched_barrier(0);
    STAMP(6);
#pragma unroll
    for (int j = 0; j < NG; ++j)
#pragma unroll
      for (int t = 0; t < 3; ++t) acc[t] = mfma(pa[j], vr[3 * j + t], acc[t]);
    if ((lane >> 5) == ((lane >> 3) & 1)) {
      float* pp = P + (2 * wave + (lane >> 5)) * PSTR + 8 * c7 + 4 * ((lane >> 4) & 1);
#pragma unroll
      for (int t = 0; t < 3; ++t) *(f4*)(pp + 64 * t) = acc[t];
    }
  }
  STAMP(7);
  const int rq_ = s & 3, ch = s >> 2;
  if (wave >= 3 && wave < 8) {
    const int lc = (b & 7) * 5 + (jb >> 2), qd = jb & 3;
    const int fo = (lane >> 1) * G2_STRIDE + (lane & 1) * 16;
    const int fs = WS_G2 + (lc / 20) * 128 * G2_STRIDE + (lc % 20) * 128 + qd * 32;
    v4u g[4];
    while (__hip_atomic_load(&ctl[1], __ATOMIC_RELAXED, __HIP_MEMORY_SCOPE_WORKGROUP) < WO_GATE) __builtin_amdgcn_s_sleep(2);
    asm volatile("" ::: "memory");
    const rsrc_t ro = mk_rsrc(wo + (size_t)(h * DH) * D + 320 * ch + 64 * (wave - 3), DH * D * 2);
    v4u wr[6];
#pragma unroll
    for (int j = 0; j < 6; ++j) {
      const int n = 3 * rq_ + (j >> 1);
      wr[j] = __builtin_amdgcn_raw_buffer_load_b128(ro, r8 * (4 * D * 2) + c7 * 16, (32 * (n >> 1) + 2 * (n & 1) + (j & 1)) * D * 2, AUX_NT);
    }
    STAMP(12);
    BARRIER();
    STAMP(8);
    BARRIER();
    BARRIER();
    STAMP(10);
    const v4u oa = *(const v4u*)(os + 8 * r8);
    const v2u ob = *(const v2u*)(os + 8 * r8 + 4);
    f4 y = {0.f, 0.f, 0.f, 0.f};
#pragma unroll
    for (int j = 0; j < 6; ++j) y = mfma(msk & (j < 4 ? oa[j] : ob[j - 4]), wr[j], y);
    float y0, y1; fold_tile(y, y0, y1);
    if ((lane & 8) == 0)
      __builtin_amdgcn_raw_buffer_store_b128(v4u{asu(y0), tag, asu(y1), tag}, rws, (lane >> 4) * 128 + c7 * 16, WS_G2 + ((ch * 32 + h) * 4 + rq_) * G2_STRIDE + (wave - 3) * 512, AUX_SC1);
    STAMP(13);
    if (wave == 3 && jb < 20) {
      bool ok[4] = {false, false, false, false};
      _Pragma("nounroll") for (unsigned spin = 0; spin < SPIN_CAP; ++spin) {
#pragma unroll
        for (int i = 0; i < 4; ++i) if (!ok[i]) g[i] = __builtin_amdgcn_raw_buffer_load_b128(rws, fo, fs + i * 32 * G2_STRIDE, AUX_SC1);
#pragma unroll
        for (int i = 0; i < 4; ++i) ok[i] = tagged(g[i], tag);
        if (__all(ok[0] & ok[1] & ok[2] & ok[3])) break;
        asm volatile("" ::: "memory");
      }
      STAMP(14);
      float a0 = (asf(g[0][0]) + asf(g[1][0])) + (asf(g[2][0]) + asf(g[3][0]));
      float a1 = (asf(g[0][2]) + asf(g[1][2])) + (asf(g[2][2]) + asf(g[3][2]));
      swap32(a0, a1); a0 += a1;
      { float a2 = a0; swap16(a0, a2); a0 += a2; }
      a0 += dpp<ROR8>(a0); a0 += dpp<ROR4>(a0); a0 += dpp<XOR2>(a0);
      if ((lane & 30) == 0) out[64 * (lc >> 2) + 8 * (2 * qd + (lane & 1)) + 4 * (lc & 1) + (lc & 2) + (lane >> 5)] = (u16)pk_bf16(a0, a0);
      if (b == 0 && lane == 0) __builtin_amdgcn_raw_buffer_store_b32(tag, rws, 0, WS_EPOCH, AUX_SC1);
    }
  } else if (wave == 0) {
    const int go = r8 * G1_STRIDE + c7 * 16, gs = WS_G1 + h * 8 * G1_STRIDE;
    v4u g[4];
    BARRIER();
    STAMP(8);
    BARRIER();
    STAMP(9);
    bool ok = false;
    _Pragma("nounroll") for (unsigned spin = 0; spin < SPIN_CAP; ++spin) {
      if (!ok) {
#pragma unroll
        for (int i = 0; i < 3; ++i) g[i] = __builtin_amdgcn_raw_buffer_load_b128(rws, go + i * 128, gs + 3 * rq_ * 128, AUX_SC1);
        g[3] = __builtin_amdgcn_raw_buffer_load_b128(rws, go + 12 * 128, gs, AUX_SC1);
        ok = tagged(g[0], tag) & tagged(g[1], tag) & tagged(g[2], tag) & tagged(g[3], tag);
      }
      if (__all(ok)) break;
      asm volatile("" ::: "memory");
    }
    STAMP(10);
    const float Ms = asf(g[3][0]), Mg = max_hi(Ms);
    const float wt = __builtin_amdgcn_exp2f(Ms - Mg);
    const float inv = 1.f / sum_hi(wt * asf(g[3][2]));
    float v0 = wt * asf(g[0][0]), v1 = wt * asf(g[0][2]), v2 = wt * asf(g[1][0]), v3 = wt * asf(g[1][2]), v4 = wt * asf(g[2][0]), v5 = wt * asf(g[2][2]);
    swap32(v0, v2); v0 += v2;
    swap32(v1, v3); v1 += v3;
    swap32(v4, v5); v4 += v5;
    swap16(v0, v1); v0 += v1;
    v5 = v4; swap16(v4, v5); v4 += v5;
    v0 += dpp<ROR8>(v0); v4 += dpp<ROR8>(v4);
    v0 *= inv; v4 *= inv;
    if ((lane & 8) == 0) { os[8 * c7 + (lane >> 4)] = pk_bf16(v0, v0); os[8 * c7 + 4 + (lane >> 5)] = pk_bf16(v4, v4); }
    BARRIER();
  } else {
    BARRIER();
    STAMP(8);
    const int g1 = WS_G1 + (h * 8 + s) * G1_STRIDE;
    if (wave >= 8 && wave < 14) {
      const int w = wave - 8;
      const float* pp = P + 4 * r8 * PSTR + 32 * w + 4 * c7;
      const f2 mw = *(const f2*)(ms + 2 * r8);
      const f4 u0 = *(const f4*)pp + *(const f4*)(pp + PSTR), u1 = *(const f4*)(pp + 2 * PSTR) + *(const f4*)(pp + 3 * PSTR);
      const float M = max_hi(fmaxf(mw[0], mw[1]));
      const f4 u = u0 * __builtin_amdgcn_exp2f(mw[0] - M) + u1 * __builtin_amdgcn_exp2f(mw[1] - M);
      float a = u[0], b2 = u[1], c = u[2], e = u[3];
      swap32(a, c); a += c;
      swap32(b2, e); b2 += e;
      c = a; swap16(a, c); a += c;
      e = b2; swap16(b2, e); b2 += e;
      a += dpp<ROR8>(a); b2 += dpp<ROR8>(b2);
      if ((lane & 24) == 0) __builtin_amdgcn_raw_buffer_store_b128(v4u{asu(a), tag, asu(b2), tag}, rws, (lane >> 5) * 128 + c7 * 16, g1 + w * 256, AUX_SC1);
    } else if (wave == 14) {
      const float mw = ms[lane & 15];
      float M = max8(mw); M = fmaxf(M, dpp<ROR8>(M));
      float L = sum8(ls[lane & 15] * __builtin_amdgcn_exp2f(mw - M)); L += dpp<ROR8>(L);
      if (lane < 8) __builtin_amdgcn_raw_buffer_store_b128(v4u{asu(M), tag, asu(L), tag}, rws, lane * 16, g1 + 12 * 128, AUX_SC1);
    }
    STAMP(9);
    BARRIER();
    BARRIER();
  }
  STAMP(11);
  if (stv[1] != 0 && stv[0] != 0 && (true)) { const u64 tend = stv[0] + 4000 + 0 * (stv[1] - stv[0]); u64 tn;
    do { __builtin_amdgcn_s_sleep(1); asm volatile("s_memrealtime %0\n\ts_waitcnt lgkmcnt(0)" : "=s"(tn) :: "memory"); } while (tn < tend && tn < stv[0] + 100000); }
}

extern "C" void kernel_launch(void* const* d_in, const int* in_sizes, int n_in, void* d_out, int out_size, void* d_ws, size_t ws_size, hipStream_t stream) {
  fused_decode<<<256, NW * 64, 0, stream>>>((const u16*)d_in[0], (const u16*)d_in[1], (const u16*)d_in[2], (const u16*)d_in[3], (const u16*)d_in[4], (u16*)d_out, (unsigned char*)d_ws
  );
}
```

```cpp
#define WIN 3
#include <hip/hip_runtime.h>
#include <hip/hip_bf16.h>
#include <stdint.h>

typedef unsigned short u16;
typedef unsigned long long u64;
typedef unsigned v4u __attribute__((ext_vector_type(4)));
typedef unsigned v2u __attribute__((ext_vector_type(2)));
typedef float f4 __attribute__((ext_vector_type(4)));
typedef float f2 __attribute__((ext_vector_type(2)));
typedef __bf16 bf16x2 __attribute__((ext_vector_type(2)));
typedef __bf16 bf16x8 __attribute__((ext_vector_type(8)));
typedef __amdgpu_buffer_rsrc_t rsrc_t;
#define LDSP __attribute__((address_space(3)))

#define CODEPF 9024
#define PRETOUCH 3
#define TLBPF 0
#define N_QB 20
#define N_WO 47
#define QDLY 70
#define MDLY 80
#define QWIN 20
#define AUX_OUT 0

constexpr int D = 640, H = 32, SK = 32768, DH = 192, W = 4096;
constexpr int NW = 16, NG = 8;
constexpr int AUX_NT = 2, AUX_SC1 = 16;
constexpr int WS_EPOCH = 0;
constexpr int WS_GQ = 4096;
constexpr int GQ_STRIDE = 10 * 512;
constexpr int WS_G1 = WS_GQ + 32 * GQ_STRIDE;
constexpr int G1_STRIDE = 13 * 128;
constexpr int WS_G2 = WS_G1 + 256 * G1_STRIDE;
constexpr int G2_STRIDE = 21 * 128;
constexpr int WS_END = WS_G2 + 256 * G2_STRIDE;
constexpr int OFF = 0x7ffffff0;
constexpr int PSTR = 208;
constexpr unsigned SPIN_CAP = 1u << 16;

#define STAMP_MASK 0x7fff
#define STAMP(i)

__device__ __forceinline__ rsrc_t mk_rsrc(const void* p, int bytes) { return __builtin_amdgcn_make_buffer_rsrc((void*)p, 0, bytes, 0x00020000); }
__device__ __forceinline__ float asf(unsigned u) { return __builtin_bit_cast(float, u); }
__device__ __forceinline__ unsigned asu(float f) { return __builtin_bit_cast(unsigned, f); }
template <int CTRL> __device__ __forceinline__ float dpp(float x) { return asf(__builtin_amdgcn_mov_dpp(asu(x), CTRL, 0xf, 0xf, true)); }
constexpr int XOR1 = 0xB1, XOR2 = 0x4E, HMIR = 0x141, ROR8 = 0x128, ROR4 = 0x124;
__device__ __forceinline__ unsigned pk_bf16(float lo, float hi) { bf16x2 r = {(__bf16)lo, (__bf16)hi}; return __builtin_bit_cast(unsigned, r); }
__device__ __forceinline__ float dot2(unsigned a, unsigned b, float c) { return __builtin_amdgcn_fdot2_f32_bf16(__builtin_bit_cast(bf16x2, a), __builtin_bit_cast(bf16x2, b), c, false); }
__device__ __forceinline__ f4 mfma(v4u a, v4u b, f4 c) { return __builtin_amdgcn_mfma_f32_16x16x32_bf16(__builtin_bit_cast(bf16x8, a), __builtin_bit_cast(bf16x8, b), c, 0, 0, 0); }
__device__ __forceinline__ float sum8(float x) { x += dpp<XOR1>(x); x += dpp<XOR2>(x); x += dpp<HMIR>(x); return x; }
__device__ __forceinline__ float max8(float x) { x = fmaxf(x, dpp<XOR1>(x)); x = fmaxf(x, dpp<XOR2>(x)); x = fmaxf(x, dpp<HMIR>(x)); return x; }
__device__ __forceinline__ void swap32(float& a, float& b) { auto r = __builtin_amdgcn_permlane32_swap(asu(a), asu(b), false, false); a = asf(r[0]); b = asf(r[1]); }
__device__ __forceinline__ void swap16(float& a, float& b) { auto r = __builtin_amdgcn_permlane16_swap(asu(a), asu(b), false, false); a = asf(r[0]); b = asf(r[1]); }
__device__ __forceinline__ float sum_hi(float x) { x += dpp<ROR8>(x); float y = x; swap16(x, y); x += y; y = x; swap32(x, y); return x + y; }
__device__ __forceinline__ float max_hi(float x) { x = fmaxf(x, dpp<ROR8>(x)); float y = x; swap16(x, y); x = fmaxf(x, y); y = x; swap32(x, y); return fmaxf(x, y); }
#define SB() __builtin_amdgcn_sched_barrier(0)
#define BARRIER() do { SB(); asm volatile("s_waitcnt lgkmcnt(0)\n\ts_barrier" ::: "memory"); SB(); } while (0)
#define PIN(x) asm volatile("" : "+v"(x))
#define SPIN(x) asm volatile("" : "+s"(x))
__device__ __forceinline__ unsigned miss(v4u g, unsigned tag) { return (g[1] ^ tag) | (g[3] ^ tag); }
__device__ __forceinline__ bool none(unsigned x) { return __builtin_amdgcn_ballot_w64(x != 0) == 0; }

__device__ __forceinline__ void fold_tile(f4 d, float& o0, float& o1) {
  float a = d[0], b = d[2], c = d[1], e = d[3];
  swap32(a, b); swap32(c, e);
  o0 = a + dpp<ROR8>(b); o1 = c + dpp<ROR8>(e);
}

__global__ void __launch_bounds__(NW * 64) fused_decode(const u16* x_, const u16* kc_, const u16* vc_, const u16* wq_, const u16* wo_, u16* out_, unsigned char* ws_, u64* stamps) {
  __shared__ __attribute__((aligned(16))) float lds[16 * PSTR + 96 + 64 + 8 + 8 + 4];
  LDSP float* P = (LDSP float*)lds;
  LDSP unsigned* qs = (LDSP unsigned*)(P + 16 * PSTR);
  LDSP unsigned* os = qs + 96;
  LDSP float* ms = P + 16 * PSTR + 96 + 64;
  LDSP float* ls = ms + 8;
  LDSP unsigned* ctl = (LDSP unsigned*)(ls + 8);
#define FINISH(nbar) __builtin_amdgcn_endpgm()
  STAMP(0);
  asm volatile("s_load_dwordx16 s[64:79], %0, 0x0" :: "s"(__builtin_amdgcn_kernarg_segment_ptr())
               : "s64", "s65", "s66", "s67", "s68", "s69", "s70", "s71", "s72", "s73", "s74", "s75", "s76", "s77", "s78", "s79");
  unsigned pf = 0;
  if (CODEPF && threadIdx.x < 128) {
    u64 pc; asm volatile("s_getpc_b64 %0" : "=s"(pc));
    pf = __builtin_amdgcn_raw_buffer_load_b32(mk_rsrc((void*)(pc & ~127ull), CODEPF), threadIdx.x * 128 + 128, 0, 0);
  }
  SB();
#define ARG3(p0, i0, p1, i1, p2, i2) u64 p0, p1, p2; \
  asm volatile("s_waitcnt lgkmcnt(0)\n\ts_mov_b64 %0, s[%c3:%c4]\n\ts_mov_b64 %1, s[%c5:%c6]\n\ts_mov_b64 %2, s[%c7:%c8]" : "=s"(p0), "=s"(p1), "=s"(p2) \
               : "i"(64 + 2 * i0), "i"(65 + 2 * i0), "i"(64 + 2 * i1), "i"(65 + 2 * i1), "i"(64 + 2 * i2), "i"(65 + 2 * i2)); STAMP(12)
  const int b = blockIdx.x, jb = b >> 3;
  const int h = (b & 7) * 4 + (jb >> 3), s = jb & 7;
  const int tid = threadIdx.x, wave = __builtin_amdgcn_readfirstlane(tid >> 6), lane = tid & 63, r8 = lane >> 3, c7 = lane & 7;
#define MAKE_MSK() v4u msk; _Pragma("unroll") for (int d = 0; d < 4; ++d) msk[d] = ((c7 >> 1) == d) ? ((c7 & 1) ? 0xffff0000u : 0x0000ffffu) : 0u

  if (wave >= 3 && wave < 8) {
    const int rq_ = s & 3, ch = s >> 2;
    BARRIER();
    ARG3(wo, 4, out, 5, ws, 6);
    const rsrc_t rws = mk_rsrc((void*)ws, WS_END), rout = mk_rsrc((void*)out, D * 2);
    const rsrc_t ro = mk_rsrc((const u16*)wo + (size_t)(h * DH) * D + 320 * ch + 64 * (wave - 3), DH * D * 2);
    MAKE_MSK();
    const int lc = (b & 7) * 5 + (jb >> 2), qd = jb & 3;
    int fo = (lane >> 1) * G2_STRIDE + (lane & 1) * 16;
    int fs[4];
#pragma unroll
    for (int i = 0; i < 4; ++i) { fs[i] = WS_G2 + ((lc / 20) * 128 + i * 32) * G2_STRIDE + (lc % 20) * 128 + qd * 32; SPIN(fs[i]); }
    int yo = (lane & 8) ? OFF : (lane >> 4) * 128 + c7 * 16;
    int ys = WS_G2 + ((ch * 32 + h) * 4 + rq_) * G2_STRIDE + (wave - 3) * 512;
    int oo = (lane & 30) ? OFF : 2 * (64 * (lc >> 2) + 8 * (2 * qd + (lane & 1)) + 4 * (lc & 1) + (lc & 2) + (lane >> 5));
    int wl = (8 * (r8 & 3) + (r8 >> 2)) * (D * 2) + c7 * 16;
    int wsf[3];
#pragma unroll
    for (int i = 0; i < 3; ++i) { const int n = 3 * rq_ + i; wsf[i] = (32 * (n >> 1) + 2 * (n & 1)) * (D * 2); SPIN(wsf[i]); }
    const LDSP unsigned* op = os + 8 * r8;
    v4u g[4];
    PIN(fo); PIN(yo); PIN(oo); PIN(wl); PIN(msk); PIN(op); SPIN(ys);
    const bool fin = wave == 4 && jb < 20;
    BARRIER();
    v4u yv; yv[1] = yv[3] = ctl[0];
    const unsigned tag = __builtin_amdgcn_readfirstlane(yv[1]);
    PIN(yv);
    if ((PRETOUCH & 2) && fin) {
#pragma unroll
      for (int i = 0; i < 4; ++i) g[i] = __builtin_amdgcn_raw_buffer_load_b128(rws, fo, fs[i], AUX_SC1);
      asm volatile("" :: "v"(g[0]), "v"(g[1]), "v"(g[2]), "v"(g[3]));
    }
    BARRIER();
    BARRIER();
    v4u wr[6];
#pragma unroll
    for (int j = 0; j < 6; ++j) wr[j] = __builtin_amdgcn_raw_buffer_load_b128(ro, wl, wsf[j >> 1] + (j & 1) * 4 * (D * 2), AUX_NT);
    STAMP(6);
    BARRIER();
    BARRIER();
    asm volatile("" :: "v"(wr[0]), "v"(wr[1]), "v"(wr[2]), "v"(wr[3]), "v"(wr[4]), "v"(wr[5]));
    STAMP(9);
    BARRIER();
    STAMP(10);
    const v4u oa = *(const LDSP v4u*)op;
    const v2u ob = *(const LDSP v2u*)(op + 4);
    f4 y = {0.f, 0.f, 0.f, 0.f};
#pragma unroll
    for (int j = 0; j < 6; ++j) y = mfma(msk & (j < 4 ? oa[j] : ob[j - 4]), wr[j], y);
    float y0, y1; fold_tile(y, y0, y1);
    yv[0] = asu(y0); yv[2] = asu(y1);
    __builtin_amdgcn_raw_buffer_store_b128(yv, rws, yo, ys, AUX_SC1);
    STAMP(13);
    if (fin) {
      unsigned bad = 1;
      _Pragma("nounroll") for (unsigned spin = 0; spin < SPIN_CAP; ++spin) {
        if (bad) {
#pragma unroll
          for (int i = 0; i < 4; ++i) g[i] = __builtin_amdgcn_raw_buffer_load_b128(rws, fo, fs[i], AUX_SC1);
          bad = miss(g[0], tag) | miss(g[1], tag) | miss(g[2], tag) | miss(g[3], tag);
        }
        if (none(bad)) break;
        asm volatile("" ::: "memory");
      }
      STAMP(14);
      float a0 = (asf(g[0][0]) + asf(g[1][0])) + (asf(g[2][0]) + asf(g[3][0]));
      float a1 = (asf(g[0][2]) + asf(g[1][2])) + (asf(g[2][2]) + asf(g[3][2]));
      swap32(a0, a1); a0 += a1;
      { float a2 = a0; swap16(a0, a2); a0 += a2; }
      a0 += dpp<ROR8>(a0); a0 += dpp<ROR4>(a0); a0 += dpp<XOR2>(a0);
      __builtin_amdgcn_raw_buffer_store_b16((u16)pk_bf16(a0, a0), rout, oo, 0, AUX_OUT);
    }
    FINISH(0);
  }
  if (wave < 3) {
    const int rq_ = s & 3;
    const int u = 3 * s + wave;
    int wl = r8 * 10 * (H * DH * 2) + c7 * 16, xl = 20 * r8 + (u & 7) * 160;
    unsigned us = (u & 7) * 80 * (H * DH * 2) + (u >> 3) * 128 + h * (DH * 2);
    PIN(wl); PIN(xl); SPIN(us);
    ARG3(wq, 3, x, 0, ws, 6);
    const rsrc_t rq = mk_rsrc((const char*)wq + us, 80 * H * DH * 2), rx = mk_rsrc((void*)x, D * 2), rws = mk_rsrc((void*)ws, WS_END);
    v4u wr[10], g[5];
#pragma unroll
    for (int j = 0; j < 10; ++j) wr[j] = __builtin_amdgcn_raw_buffer_load_b128(rq, wl, j * (H * DH * 2), AUX_NT);
    const v4u xa = __builtin_amdgcn_raw_buffer_load_b128(rx, xl, 0, 0);
    const unsigned xb = __builtin_amdgcn_raw_buffer_load_b32(rx, xl + 16, 0, 0);
    const unsigned ep = __builtin_amdgcn_raw_buffer_load_b32(rws, wave ? OFF : 0, WS_EPOCH, AUX_SC1);
    if (tid == 0) ctl[2] = 0;
    BARRIER();
    STAMP(1);
    MAKE_MSK();
    f4 acc[3];
    acc[0] = f4{0.f, 0.f, 0.f, 0.f};
#pragma unroll
    for (int j = 0; j < 10; ++j) {
      const unsigned xw = j < 8 ? xa[j >> 1] : xb;
      const unsigned xx = __builtin_amdgcn_perm(xw, xw, (j & 1) ? 0x03020302u : 0x01000100u);
      acc[0] = mfma(msk & xx, wr[j], acc[0]);
    }
    asm volatile("" :: "v"(pf));
    if (wave) {
      *(LDSP f4*)(P + (wave - 1) * 256 + lane * 4) = acc[0];
      asm volatile("s_waitcnt lgkmcnt(0)" ::: "memory");
      if (lane == 0) __hip_atomic_fetch_add(ctl + 2, 1u, __ATOMIC_RELAXED, __HIP_MEMORY_SCOPE_WORKGROUP);
      if (wave == 2 || b != 0) FINISH(6);
      BARRIER();
      const unsigned tag = __builtin_amdgcn_readfirstlane(ctl[0]);
      BARRIER();
      __builtin_amdgcn_s_sleep(MDLY);
      _Pragma("nounroll") for (unsigned spin = 0; spin < SPIN_CAP; ++spin) {
        const v4u ge = __builtin_amdgcn_raw_buffer_load_b128(rws, (lane & 31) * 8 * G1_STRIDE, WS_G1 + 12 * 128, AUX_SC1);
        if (none(miss(ge, tag))) break;
        __builtin_amdgcn_s_sleep(8);
        asm volatile("" ::: "memory");
      }
      __builtin_amdgcn_raw_buffer_store_b32(tag, rws, lane ? OFF : 0, WS_EPOCH, AUX_SC1);
      FINISH(4);
    }
    while (__hip_atomic_load(ctl + 2, __ATOMIC_RELAXED, __HIP_MEMORY_SCOPE_WORKGROUP) != 2u) __builtin_amdgcn_s_sleep(1);
    asm volatile("" ::: "memory");
    acc[1] = *(const LDSP f4*)(P + lane * 4); acc[2] = *(const LDSP f4*)(P + 256 + lane * 4);
    const unsigned tag = __builtin_amdgcn_readfirstlane(ep) * 0x9E3779B1u + 0x7F4A7C15u;
    int gqs = WS_GQ + h * GQ_STRIDE;
    SPIN(gqs);
    const int rec = s + (s > 2) + (s > 5);
    const int to = (lane & 8) ? OFF : (lane >> 4) * 128 + c7 * 16;
    float q0, q1;
    if (s == 2 || s == 5) {
      fold_tile(s == 2 ? acc[2] : acc[1] + acc[2], q0, q1);
      __builtin_amdgcn_raw_buffer_store_b128(v4u{asu(q0), tag, asu(q1), tag}, rws, to, gqs + (rec + 1) * 512, AUX_SC1);
      fold_tile(s == 2 ? acc[0] + acc[1] : acc[0], q0, q1);
    } else fold_tile((acc[0] + acc[1]) + acc[2], q0, q1);
    STAMP(2);
    __builtin_amdgcn_raw_buffer_store_b128(v4u{asu(q0), tag, asu(q1), tag}, rws, to, gqs + rec * 512, AUX_SC1);
    SB();
    __builtin_amdgcn_s_sleep(QDLY);
    _Pragma("nounroll") for (unsigned spin = 0; spin < SPIN_CAP; ++spin) {
#pragma unroll
      for (int i = 0; i < 5; ++i) g[i] = __builtin_amdgcn_raw_buffer_load_b128(rws, lane * 16, gqs + i * 1024, AUX_SC1);
      if (none(miss(g[0], tag) | miss(g[1], tag) | miss(g[2], tag) | miss(g[3], tag) | miss(g[4], tag))) break;
      asm volatile("" ::: "memory");
    }
    {
      const bool hi = lane >= 32;
      float qa[3][2];
#pragma unroll
      for (int e = 0; e < 2; ++e) {
        qa[0][e] = asf(g[0][2 * e]) + (hi ? 0.f : asf(g[1][2 * e]));
        qa[1][e] = asf(g[2][2 * e]) + (hi ? asf(g[1][2 * e]) : asf(g[3][2 * e]));
        qa[2][e] = asf(g[4][2 * e]) + (hi ? asf(g[3][2 * e]) : 0.f);
      }
#pragma unroll
      for (int t = 0; t < 3; ++t) {
        float a0 = qa[t][0], a1 = qa[t][1], b0 = a0, b1 = a1;
        swap32(a0, b0); swap32(a1, b1);
        qs[32 * t + 4 * c7 + 2 * (r8 & 1) + ((r8 >> 1) & 1)] = pk_bf16(a0 + b0, a1 + b1);
      }
    }
    if (lane == 0) ctl[0] = tag;
    STAMP(3);
    BARRIER();
    int g1o = r8 * G1_STRIDE + c7 * 16, g1s = WS_G1 + h * 8 * G1_STRIDE, g1q = g1s + 3 * rq_ * 128;
    SPIN(g1s); SPIN(g1q); PIN(g1o);
    if (PRETOUCH & 1) {
#pragma unroll
      for (int i = 0; i < 3; ++i) g[i] = __builtin_amdgcn_raw_buffer_load_b128(rws, g1o + i * 128, g1q, AUX_SC1);
      asm volatile("" :: "v"(g[0]), "v"(g[1]), "v"(g[2]));
    }
    BARRIER();
    const float mw = ms[c7], M = max8(mw);
    const float L = sum8(ls[c7] * __builtin_amdgcn_exp2f(mw - M));
    __builtin_amdgcn_raw_buffer_store_b128(v4u{asu(M), tag, asu(L), tag}, rws, lane < 8 ? lane * 16 : OFF, g1s + s * G1_STRIDE + 12 * 128, AUX_SC1);
    SB();
    __builtin_amdgcn_s_sleep(MDLY);
    _Pragma("nounroll") for (unsigned spin = 0; spin < SPIN_CAP; ++spin) {
      g[3] = __builtin_amdgcn_raw_buffer_load_b128(rws, g1o + 12 * 128, g1s, AUX_SC1);
      if (none(miss(g[3], tag))) break;
      asm volatile("" ::: "memory");
    }
    const float Ms = asf(g[3][0]), Mg = max_hi(Ms);
    const float Lg = sum_hi(__builtin_amdgcn_exp2f(Ms - Mg) * asf(g[3][2]));
    if (lane == 0) ctl[1] = asu(__builtin_amdgcn_exp2f(M - Mg) / Lg);
    LDSP unsigned* ow0 = os + 8 * c7 + (lane >> 4); LDSP unsigned* ow1 = os + 8 * c7 + 4 + (lane >> 5);
    PIN(ow0); PIN(ow1);
    STAMP(6);
    BARRIER();
    BARRIER();
    STAMP(8);
    BARRIER();
    STAMP(9);
    _Pragma("nounroll") for (unsigned spin = 0; spin < SPIN_CAP; ++spin) {
#pragma unroll
      for (int i = 0; i < 3; ++i) g[i] = __builtin_amdgcn_raw_buffer_load_b128(rws, g1o + i * 128, g1q, AUX_SC1);
      if (none(miss(g[0], tag) | miss(g[1], tag) | miss(g[2], tag))) break;
      asm volatile("" ::: "memory");
    }
    STAMP(10);
    float v0 = asf(g[0][0]), v1 = asf(g[0][2]), v2 = asf(g[1][0]), v3 = asf(g[1][2]), v4 = asf(g[2][0]), v5 = asf(g[2][2]);
    swap32(v0, v2); v0 += v2;
    swap32(v1, v3); v1 += v3;
    swap32(v4, v5); v4 += v5;
    swap16(v0, v1); v0 += v1;
    v5 = v4; swap16(v4, v5); v4 += v5;
    v0 += dpp<ROR8>(v0); v4 += dpp<ROR8>(v4);
    *ow0 = pk_bf16(v0, v0); *ow1 = pk_bf16(v4, v4);
    BARRIER();
    FINISH(0);
  }
  {
    const int w = wave - 8;
    unsigned kvo = (unsigned)(h * SK + (SK - W) + (w * 8 + s) * 8) * (DH * 2);
    int lo = r8 * (DH * 2) + c7 * 16;
    PIN(lo); SPIN(kvo);
    ARG3(kc, 1, vc, 2, ws, 6);
    const rsrc_t rk = mk_rsrc((const char*)kc + kvo, (7 * 512 + 8) * DH * 2), rv = mk_rsrc((const char*)vc + kvo, (7 * 512 + 8) * DH * 2);
    v4u kv[48];
#define ISSUE(n) kv[n] = __builtin_amdgcn_raw_buffer_load_b128((n) < 24 ? rk : rv, lo + ((n) % 3) * 128, (((n) % 24) / 3) * 512 * DH * 2, AUX_NT)
#define STEP(n) do { SB(); asm volatile("s_waitcnt vmcnt(%0)" :: "n"(WIN - 1) : "memory"); ISSUE(n); SB(); } while (0)
#pragma unroll
    for (int n = 0; n < WIN; ++n) ISSUE(n);
    STAMP(1);
    BARRIER();
#pragma unroll
    for (int n = WIN; n <= N_QB; ++n) STEP(n);
    STAMP(3);
    BARRIER();
    STAMP(4);
    v4u qv[3];
#pragma unroll
    for (int t = 0; t < 3; ++t) qv[t] = *(const LDSP v4u*)(qs + 32 * t + 4 * c7);
    const unsigned tag = ctl[0];
    constexpr float C2 = 0.07216878364870322f * 1.4426950408889634f;
    constexpr int N_SC = (N_QB + 7 > 23 + WIN) ? N_QB + 7 : 23 + WIN;
    float sc[NG];
#pragma unroll
    for (int n = N_QB; n <= N_SC; ++n) {
      if (n > N_QB) STEP(n);
#pragma unroll
      for (int j = 0; j < NG; ++j) {
        if (n == ((N_QB + j > 3 * j + 2 + WIN) ? N_QB + j : 3 * j + 2 + WIN)) {
          float a = 0.f;
#pragma unroll
          for (int t = 0; t < 3; ++t)
#pragma unroll
            for (int k = 0; k < 4; ++k) a = dot2(kv[3 * j + t][k], qv[t][k], a);
          sc[j] = sum8(a) * C2;
          PIN(sc[j]);
        }
      }
    }
    float m = sc[0];
#pragma unroll
    for (int j = 1; j < NG; ++j) m = fmaxf(m, sc[j]);
    m = max_hi(m);
    STEP(N_SC + 1);
    float l = 0.f;
#pragma unroll
    for (int j = 0; j < NG; ++j) { sc[j] = __builtin_amdgcn_exp2f(sc[j] - m); l += sc[j]; }
    l = sum_hi(l);
    if (lane == 0) { ms[w] = m; ls[w] = l; }
    STEP(N_SC + 2);
    STAMP(5);
    BARRIER();
    const float wt = __builtin_amdgcn_exp2f(m - max8(ms[c7]));
    MAKE_MSK();
    f4 acc[3] = {{0.f, 0.f, 0.f, 0.f}, {0.f, 0.f, 0.f, 0.f}, {0.f, 0.f, 0.f, 0.f}};
    v4u pa;
#define PV(n) do { if ((n) % 3 == 0) { const float p = sc[((n) - 24) / 3] * wt; pa = msk & pk_bf16(p, p); } acc[(n) % 3] = mfma(pa, kv[n], acc[(n) % 3]); } while (0)
#pragma unroll
    for (int n = 24; n <= N_SC + 2 - WIN; ++n) PV(n);
#pragma unroll
    for (int n = N_SC + 3; n < 48; ++n) { STEP(n); if (n == N_WO) BARRIER(); PV(n - WIN); }
    STAMP(6);
    LDSP float* pp = P + (2 * w + (lane >> 5)) * PSTR + 8 * c7 + 4 * ((lane >> 4) & 1);
    const bool pvalid = (lane >> 5) == ((lane >> 3) & 1);
    const LDSP float* rp = P + 2 * r8 * PSTR + 32 * w + 4 * c7;
    const rsrc_t rws = mk_rsrc((void*)ws, WS_END);
    int so = (lane & 8) ? OFF : (lane >> 4) * 64 + c7 * 8;
    int ss = WS_G1 + (h * 8 + s) * G1_STRIDE + w * 256;
    float f = asf(ctl[1]);
    v2u sv; sv[1] = tag;
    PIN(so); SPIN(ss); PIN(f); PIN(pp); PIN(rp); PIN(sv);
#pragma unroll
    for (int n = 48 - WIN; n < 45; ++n) PV(n);
#pragma unroll
    for (int n = 45; n < 48; ++n) { if (n >= 48 - WIN) PV(n); if (pvalid) *(LDSP f4*)(pp + 64 * (n - 45)) = acc[n - 45]; SB(); }
    STAMP(7);
    BARRIER();
    STAMP(8);
    if (w < 6) {
      const f4 u = *(const LDSP f4*)rp + *(const LDSP f4*)(rp + PSTR);
      float a = u[0], b2 = u[1], c = u[2], e = u[3];
      swap32(a, c); a += c;
      swap32(b2, e); b2 += e;
      swap16(a, b2); a += b2;
      a += dpp<ROR8>(a);
      sv[0] = asu(a * f);
      __builtin_amdgcn_raw_buffer_store_b64(sv, rws, so, ss, AUX_SC1);
    }
    STAMP(9);
    BARRIER();
    FINISH(1);
  }
}

extern "C" void kernel_launch(void* const* d_in, const int* in_sizes, int n_in, void* d_out, int out_size, void* d_ws, size_t ws_size, hipStream_t stream) {
  fused_decode<<<256, NW * 64, 0, stream>>>((const u16*)d_in[0], (const u16*)d_in[1], (const u16*)d_in[2], (const u16*)d_in[3], (const u16*)d_in[4], (u16*)d_out, (unsigned char*)d_ws,
                                            (u64*)((char*)d_ws + (64 << 20)));
}
```

```cpp
#include <hip/hip_runtime.h>
#include <hip/hip_bf16.h>
#include <stdint.h>

typedef unsigned short u16;
typedef unsigned long long u64;
typedef unsigned v4u __attribute__((ext_vector_type(4)));
typedef unsigned v2u __attribute__((ext_vector_type(2)));
typedef float f4 __attribute__((ext_vector_type(4)));
typedef float f2 __attribute__((ext_vector_type(2)));
typedef __bf16 bf16x2 __attribute__((ext_vector_type(2)));
typedef __bf16 bf16x8 __attribute__((ext_vector_type(8)));
typedef __amdgpu_buffer_rsrc_t rsrc_t;
#define LDSP __attribute__((address_space(3)))

#define PRELOAD 0
#define P1 0
#define P2 0
#define PSL 2
#define H1DLY 6
#define H2DLY 12
#define PRIO 0
#define S1 0
#define S2 0
#define XTRA 0
#define WINV WIN
#define WINE WINV
#define NE 42
#define WIN0 WIN
#define BURST 1
#define CODEPF 7808
#define PRETOUCH 3
#define TLBPF 0
#define WIN 3
#define N_QB 20
#define N_WO 47
#define QDLY 70
#define MDLY 80
#define QWIN 20
#define AUX_OUT 0

constexpr int D = 640, H = 32, SK = 32768, DH = 192, W = 4096;
constexpr int NW = 16, NG = 8;
constexpr int AUX_NT = 2, AUX_SC1 = 16;
constexpr int WS_EPOCH = 0;
constexpr int WS_GQ = 4096;
constexpr int GQ_STRIDE = 10 * 512;
constexpr int WS_G1 = WS_GQ + 32 * GQ_STRIDE;
constexpr int G1_STRIDE = 13 * 128;
constexpr int WS_G2 = WS_G1 + 256 * G1_STRIDE;
constexpr int G2_STRIDE = 21 * 128;
constexpr int WS_END = WS_G2 + 256 * G2_STRIDE;
constexpr int OFF = 0x7ffffff0;
constexpr int PSTR = 208;
constexpr unsigned SPIN_CAP = 1u << 16;

#define STAMP_MASK 0x7fff
#define STAMP(i)

__device__ __forceinline__ rsrc_t mk_rsrc(const void* p, int bytes) { return __builtin_amdgcn_make_buffer_rsrc((void*)p, 0, bytes, 0x00020000); }
__device__ __forceinline__ float asf(unsigned u) { return __builtin_bit_cast(float, u); }
__device__ __forceinline__ unsigned asu(float f) { return __builtin_bit_cast(unsigned, f); }
template <int CTRL> __device__ __forceinline__ float dpp(float x) { return asf(__builtin_amdgcn_mov_dpp(asu(x), CTRL, 0xf, 0xf, true)); }
constexpr int XOR1 = 0xB1, XOR2 = 0x4E, HMIR = 0x141, ROR8 = 0x128, ROR4 = 0x124;
__device__ __forceinline__ unsigned pk_bf16(float lo, float hi) { bf16x2 r = {(__bf16)lo, (__bf16)hi}; return __builtin_bit_cast(unsigned, r); }
__device__ __forceinline__ float dot2(unsigned a, unsigned b, float c) { return __builtin_amdgcn_fdot2_f32_bf16(__builtin_bit_cast(bf16x2, a), __builtin_bit_cast(bf16x2, b), c, false); }
__device__ __forceinline__ f4 mfma(v4u a, v4u b, f4 c) { return __builtin_amdgcn_mfma_f32_16x16x32_bf16(__builtin_bit_cast(bf16x8, a), __builtin_bit_cast(bf16x8, b), c, 0, 0, 0); }
__device__ __forceinline__ float sum8(float x) { x += dpp<XOR1>(x); x += dpp<XOR2>(x); x += dpp<HMIR>(x); return x; }
__device__ __forceinline__ float max8(float x) { x = fmaxf(x, dpp<XOR1>(x)); x = fmaxf(x, dpp<XOR2>(x)); x = fmaxf(x, dpp<HMIR>(x)); return x; }
__device__ __forceinline__ void swap32(float& a, float& b) { auto r = __builtin_amdgcn_permlane32_swap(asu(a), asu(b), false, false); a = asf(r[0]); b = asf(r[1]); }
__device__ __forceinline__ void swap16(float& a, float& b) { auto r = __builtin_amdgcn_permlane16_swap(asu(a), asu(b), false, false); a = asf(r[0]); b = asf(r[1]); }
__device__ __forceinline__ float sum_hi(float x) { x += dpp<ROR8>(x); float y = x; swap16(x, y); x += y; y = x; swap32(x, y); return x + y; }
__device__ __forceinline__ float max_hi(float x) { x = fmaxf(x, dpp<ROR8>(x)); float y = x; swap16(x, y); x = fmaxf(x, y); y = x; swap32(x, y); return fmaxf(x, y); }
#define SB() __builtin_amdgcn_sched_barrier(0)
#define BARRIER() do { SB(); asm volatile("s_waitcnt lgkmcnt(0)\n\ts_barrier" ::: "memory"); SB(); } while (0)
#define PIN(x) asm volatile("" : "+v"(x))
#define SPIN(x) asm volatile("" : "+s"(x))
__device__ __forceinline__ unsigned miss(v4u g, unsigned tag) { return (g[1] ^ tag) | (g[3] ^ tag); }
__device__ __forceinline__ bool none(unsigned x) { return __builtin_amdgcn_ballot_w64(x != 0) == 0; }

__device__ __forceinline__ void fold_tile(f4 d, float& o0, float& o1) {
  float a = d[0], b = d[2], c = d[1], e = d[3];
  swap32(a, b); swap32(c, e);
  o0 = a + dpp<ROR8>(b); o1 = c + dpp<ROR8>(e);
}

__global__ void __launch_bounds__(NW * 64) fused_decode(const u16* x_, const u16* kc_, const u16* vc_, const u16* wq_, const u16* wo_, u16* out_, unsigned char* ws_, u64* stamps) {
  __shared__ __attribute__((aligned(16))) float lds[16 * PSTR + 96 + 64 + 8 + 8 + 4];
  LDSP float* P = (LDSP float*)lds;
  LDSP unsigned* qs = (LDSP unsigned*)(P + 16 * PSTR);
  LDSP unsigned* os = qs + 96;
  LDSP float* ms = P + 16 * PSTR + 96 + 64;
  LDSP float* ls = ms + 8;
  LDSP unsigned* ctl = (LDSP unsigned*)(ls + 8);
#define FINISH(nbar) __builtin_amdgcn_endpgm()
  const u64 t00 = __builtin_amdgcn_s_memrealtime();
  asm volatile("s_load_dwordx16 s[64:79], %0, 0x0" :: "s"(__builtin_amdgcn_kernarg_segment_ptr())
               : "s64", "s65", "s66", "s67", "s68", "s69", "s70", "s71", "s72", "s73", "s74", "s75", "s76", "s77", "s78", "s79");
  unsigned pf = 0;
  if (CODEPF && threadIdx.x < (CODEPF > 8192 ? 128 : 64)) {
    u64 pc; asm volatile("s_getpc_b64 %0" : "=s"(pc));
    pf = __builtin_amdgcn_raw_buffer_load_b32(mk_rsrc((void*)(pc & ~127ull), CODEPF), threadIdx.x * 128 + 128, 0, 0);
  }
  SB();
#define ARG3(p0, i0, p1, i1, p2, i2) u64 p0, p1, p2; \
  asm volatile("s_waitcnt lgkmcnt(0)\n\ts_mov_b64 %0, s[%c3:%c4]\n\ts_mov_b64 %1, s[%c5:%c6]\n\ts_mov_b64 %2, s[%c7:%c8]" : "=s"(p0), "=s"(p1), "=s"(p2) \
               : "i"(64 + 2 * i0), "i"(65 + 2 * i0), "i"(64 + 2 * i1), "i"(65 + 2 * i1), "i"(64 + 2 * i2), "i"(65 + 2 * i2)); STAMP(12)
  const int b = blockIdx.x, jb = b >> 3;
  const int h = (b & 7) * 4 + (jb >> 3), s = jb & 7;
  const int tid = threadIdx.x, wave = __builtin_amdgcn_readfirstlane(tid >> 6), lane = tid & 63, r8 = lane >> 3, c7 = lane & 7;
#define MAKE_MSK() v4u msk; _Pragma("unroll") for (int d = 0; d < 4; ++d) msk[d] = ((c7 >> 1) == d) ? ((c7 & 1) ? 0xffff0000u : 0x0000ffffu) : 0u

  if (wave >= 3 && wave < 8) {
    const int rq_ = s & 3, ch = s >> 2;
    BARRIER();
    ARG3(wo, 4, out, 5, ws, 6);
    const rsrc_t rws = mk_rsrc((void*)ws, WS_END), rout = mk_rsrc((void*)out, D * 2);
    const rsrc_t ro = mk_rsrc((const u16*)wo + (size_t)(h * DH) * D + 320 * ch + 64 * (wave - 3), DH * D * 2);
    MAKE_MSK();
    const int lc = (b & 7) * 5 + (jb >> 2), qd = jb & 3;
    int fo = (lane >> 1) * G2_STRIDE + (lane & 1) * 16;
    int fs[4];
#pragma unroll
    for (int i = 0; i < 4; ++i) { fs[i] = WS_G2 + ((lc / 20) * 128 + i * 32) * G2_STRIDE + (lc % 20) * 128 + qd * 32; SPIN(fs[i]); }
    int yo = (lane & 8) ? OFF : (lane >> 4) * 128 + c7 * 16;
    int ys = WS_G2 + ((ch * 32 + h) * 4 + rq_) * G2_STRIDE + (wave - 3) * 512;
    int oo = (lane & 30) ? OFF : 2 * (64 * (lc >> 2) + 8 * (2 * qd + (lane & 1)) + 4 * (lc & 1) + (lc & 2) + (lane >> 5));
    int wl = (8 * (r8 & 3) + (r8 >> 2)) * (D * 2) + c7 * 16;
    int wsf[3];
#pragma unroll
    for (int i = 0; i < 3; ++i) { const int n = 3 * rq_ + i; wsf[i] = (32 * (n >> 1) + 2 * (n & 1)) * (D * 2); SPIN(wsf[i]); }
    const LDSP unsigned* op = os + 8 * r8;
    v4u g[4];
    PIN(fo); PIN(yo); PIN(oo); PIN(wl); PIN(msk); PIN(op); SPIN(ys);
    const bool fin = wave == 4 && jb < 20;
    if (TLBPF && wave == 7) {
      u64 vcp; asm volatile("s_mov_b64 %0, s[68:69]" : "=s"(vcp));
      const unsigned t0 = __builtin_amdgcn_raw_buffer_load_b32(mk_rsrc((const char*)vcp + (size_t)(h * SK + SK - W) * (DH * 2), W * DH * 2), lane < 7 ? lane * 262144 - (lane == 6) * 4 : OFF, 0, AUX_NT);
      const unsigned t1 = __builtin_amdgcn_raw_buffer_load_b32(ro, lane < 2 ? lane * 190 * D * 2 : OFF, 0, AUX_NT);
      const unsigned t2 = __builtin_amdgcn_raw_buffer_load_b32(rout, lane ? OFF : 0, 0, 0);
      asm volatile("" :: "v"(t0), "v"(t1), "v"(t2));
    }
    BARRIER();
    v4u yv; yv[1] = yv[3] = ctl[0];
    const unsigned tag = __builtin_amdgcn_readfirstlane(yv[1]);
    PIN(yv);
    if ((PRETOUCH & 2) && fin) {
#pragma unroll
      for (int i = 0; i < 4; ++i) g[i] = __builtin_amdgcn_raw_buffer_load_b128(rws, fo, fs[i], AUX_SC1);
      asm volatile("" :: "v"(g[0]), "v"(g[1]), "v"(g[2]), "v"(g[3]));
    }
    BARRIER();
    BARRIER();
    if (PRIO) __builtin_amdgcn_s_setprio(3);
    v4u wr[6];
#pragma unroll
    for (int j = 0; j < 6; ++j) wr[j] = __builtin_amdgcn_raw_buffer_load_b128(ro, wl, wsf[j >> 1] + (j & 1) * 4 * (D * 2), AUX_NT);
    STAMP(6);
    BARRIER();
    BARRIER();
    asm volatile("" :: "v"(wr[0]), "v"(wr[1]), "v"(wr[2]), "v"(wr[3]), "v"(wr[4]), "v"(wr[5]));
    STAMP(9);
    BARRIER();
    STAMP(10);
    const v4u oa = *(const LDSP v4u*)op;
    const v2u ob = *(const LDSP v2u*)(op + 4);
    f4 y = {0.f, 0.f, 0.f, 0.f};
#pragma unroll
    for (int j = 0; j < 6; ++j) y = mfma(msk & (j < 4 ? oa[j] : ob[j - 4]), wr[j], y);
    float y0, y1; fold_tile(y, y0, y1);
    yv[0] = asu(y0); yv[2] = asu(y1);
    __builtin_amdgcn_raw_buffer_store_b128(yv, rws, yo, ys, AUX_SC1);
    STAMP(13);
    if (fin) {
      if (H2DLY) __builtin_amdgcn_s_sleep(H2DLY);
      if (P2 == 0) {
        unsigned bad = 1;
        _Pragma("nounroll") for (unsigned spin = 0; spin < SPIN_CAP; ++spin) {
          if (bad) {
#pragma unroll
            for (int i = 0; i < 4; ++i) g[i] = __builtin_amdgcn_raw_buffer_load_b128(rws, fo, fs[i], AUX_SC1);
            bad = miss(g[0], tag) | miss(g[1], tag) | miss(g[2], tag) | miss(g[3], tag);
          }
          if (none(bad)) break;
          if (S2) __builtin_amdgcn_s_sleep(S2);
          asm volatile("" ::: "memory");
        }
      } else if (P2 == 1) {
        _Pragma("nounroll") for (unsigned spin = 0; spin < SPIN_CAP; ++spin) {
#pragma unroll
          for (int i = 0; i < 4; ++i) g[i] = __builtin_amdgcn_raw_buffer_load_b128(rws, fo, fs[i], AUX_SC1);
          if (none(miss(g[0], tag) | miss(g[1], tag) | miss(g[2], tag) | miss(g[3], tag))) break;
          asm volatile("" ::: "memory");
        }
      } else {
#pragma unroll
        for (int i = 0; i < 4; ++i) g[i] = __builtin_amdgcn_raw_buffer_load_b128(rws, fo, fs[i], AUX_SC1);
        _Pragma("nounroll") for (unsigned spin = 0; spin < SPIN_CAP; ++spin) {
          const bool n0 = none(miss(g[0], tag)), n1 = none(miss(g[1], tag)), n2 = none(miss(g[2], tag)), n3 = none(miss(g[3], tag));
          if (n0 & n1 & n2 & n3) break;
          if (!n0) g[0] = __builtin_amdgcn_raw_buffer_load_b128(rws, fo, fs[0], AUX_SC1);
          if (!n1) g[1] = __builtin_amdgcn_raw_buffer_load_b128(rws, fo, fs[1], AUX_SC1);
          if (!n2) g[2] = __builtin_amdgcn_raw_buffer_load_b128(rws, fo, fs[2], AUX_SC1);
          if (!n3) g[3] = __builtin_amdgcn_raw_buffer_load_b128(rws, fo, fs[3], AUX_SC1);
          asm volatile("" ::: "memory");
        }
      }
      STAMP(14);
      float a0 = (asf(g[0][0]) + asf(g[1][0])) + (asf(g[2][0]) + asf(g[3][0]));
      float a1 = (asf(g[0][2]) + asf(g[1][2])) + (asf(g[2][2]) + asf(g[3][2]));
      swap32(a0, a1); a0 += a1;
      { float a2 = a0; swap16(a0, a2); a0 += a2; }
      a0 += dpp<ROR8>(a0); a0 += dpp<ROR4>(a0); a0 += dpp<XOR2>(a0);
      __builtin_amdgcn_raw_buffer_store_b16((u16)pk_bf16(a0, a0), rout, oo, 0, AUX_OUT);
      if (b == 0) { const u64 te = t00 + 4000 + (0); u64 tn; do { asm volatile("s_memrealtime %0\n\ts_waitcnt lgkmcnt(0)" : "=s"(tn) :: "memory"); } while (tn < te && tn < t00 + 50000); }
    }
    FINISH(0);
  }
  if (wave < 3) {
    const int rq_ = s & 3;
    const int u = 3 * s + wave;
    int wl = r8 * 10 * (H * DH * 2) + c7 * 16, xl = 20 * r8 + (u & 7) * 160;
    unsigned us = (u & 7) * 80 * (H * DH * 2) + (u >> 3) * 128 + h * (DH * 2);
    PIN(wl); PIN(xl); SPIN(us);
    ARG3(wq, 3, x, 0, ws, 6);
    const rsrc_t rq = mk_rsrc((const char*)wq + us, 80 * H * DH * 2), rx = mk_rsrc((void*)x, D * 2), rws = mk_rsrc((void*)ws, WS_END);
    v4u wr[10], g[5];
#pragma unroll
    for (int j = 0; j < 10; ++j) wr[j] = __builtin_amdgcn_raw_buffer_load_b128(rq, wl, j * (H * DH * 2), AUX_NT);
    const v4u xa = __builtin_amdgcn_raw_buffer_load_b128(rx, xl, 0, 0);
    const unsigned xb = __builtin_amdgcn_raw_buffer_load_b32(rx, xl + 16, 0, 0);
    const unsigned ep = __builtin_amdgcn_raw_buffer_load_b32(rws, wave ? OFF : 0, WS_EPOCH, AUX_SC1);
    if (tid == 0) ctl[2] = 0;
    BARRIER();
    STAMP(1);
    MAKE_MSK();
    f4 acc[3];
    acc[0] = f4{0.f, 0.f, 0.f, 0.f};
#pragma unroll
    for (int j = 0; j < 10; ++j) {
      const unsigned xw = j < 8 ? xa[j >> 1] : xb;
      const unsigned xx = __builtin_amdgcn_perm(xw, xw, (j & 1) ? 0x03020302u : 0x01000100u);
      acc[0] = mfma(msk & xx, wr[j], acc[0]);
    }
    asm volatile("" :: "v"(pf));
    if (wave) {
      *(LDSP f4*)(P + (wave - 1) * 256 + lane * 4) = acc[0];
      asm volatile("s_waitcnt lgkmcnt(0)" ::: "memory");
      if (lane == 0) __hip_atomic_fetch_add(ctl + 2, 1u, __ATOMIC_RELAXED, __HIP_MEMORY_SCOPE_WORKGROUP);
      if (wave == 2 || b != 0) FINISH(6);
      BARRIER();
      const unsigned tag = __builtin_amdgcn_readfirstlane(ctl[0]);
      BARRIER();
      __builtin_amdgcn_s_sleep(MDLY);
      _Pragma("nounroll") for (unsigned spin = 0; spin < SPIN_CAP; ++spin) {
        const v4u ge = __builtin_amdgcn_raw_buffer_load_b128(rws, (lane & 31) * 8 * G1_STRIDE, WS_G1 + 12 * 128, AUX_SC1);
        if (none(miss(ge, tag))) break;
        __builtin_amdgcn_s_sleep(8);
        asm volatile("" ::: "memory");
      }
      __builtin_amdgcn_raw_buffer_store_b32(tag, rws, lane ? OFF : 0, WS_EPOCH, AUX_SC1);
      FINISH(4);
    }
    while (__hip_atomic_load(ctl + 2, __ATOMIC_RELAXED, __HIP_MEMORY_SCOPE_WORKGROUP) != 2u) __builtin_amdgcn_s_sleep(1);
    asm volatile("" ::: "memory");
    acc[1] = *(const LDSP f4*)(P + lane * 4); acc[2] = *(const LDSP f4*)(P + 256 + lane * 4);
    const unsigned tag = __builtin_amdgcn_readfirstlane(ep) * 0x9E3779B1u + 0x7F4A7C15u;
    int gqs = WS_GQ + h * GQ_STRIDE;
    SPIN(gqs);
    const int rec = s + (s > 2) + (s > 5);
    const int to = (lane & 8) ? OFF : (lane >> 4) * 128 + c7 * 16;
    float q0, q1;
    if (s == 2 || s == 5) {
      fold_tile(s == 2 ? acc[2] : acc[1] + acc[2], q0, q1);
      __builtin_amdgcn_raw_buffer_store_b128(v4u{asu(q0), tag, asu(q1), tag}, rws, to, gqs + (rec + 1) * 512, AUX_SC1);
      fold_tile(s == 2 ? acc[0] + acc[1] : acc[0], q0, q1);
    } else fold_tile((acc[0] + acc[1]) + acc[2], q0, q1);
    STAMP(2);
    __builtin_amdgcn_raw_buffer_store_b128(v4u{asu(q0), tag, asu(q1), tag}, rws, to, gqs + rec * 512, AUX_SC1);
    SB();
    __builtin_amdgcn_s_sleep(QDLY);
    _Pragma("nounroll") for (unsigned spin = 0; spin < SPIN_CAP; ++spin) {
#pragma unroll
      for (int i = 0; i < 5; ++i) g[i] = __builtin_amdgcn_raw_buffer_load_b128(rws, lane * 16, gqs + i * 1024, AUX_SC1);
      if (none(miss(g[0], tag) | miss(g[1], tag) | miss(g[2], tag) | miss(g[3], tag) | miss(g[4], tag))) break;
      asm volatile("" ::: "memory");
    }
    {
      const bool hi = lane >= 32;
      float qa[3][2];
#pragma unroll
      for (int e = 0; e < 2; ++e) {
        qa[0][e] = asf(g[0][2 * e]) + (hi ? 0.f : asf(g[1][2 * e]));
        qa[1][e] = asf(g[2][2 * e]) + (hi ? asf(g[1][2 * e]) : asf(g[3][2 * e]));
        qa[2][e] = asf(g[4][2 * e]) + (hi ? asf(g[3][2 * e]) : 0.f);
      }
#pragma unroll
      for (int t = 0; t < 3; ++t) {
        float a0 = qa[t][0], a1 = qa[t][1], b0 = a0, b1 = a1;
        swap32(a0, b0); swap32(a1, b1);
        qs[32 * t + 4 * c7 + 2 * (r8 & 1) + ((r8 >> 1) & 1)] = pk_bf16(a0 + b0, a1 + b1);
      }
    }
    if (lane == 0) ctl[0] = tag;
    STAMP(3);
    BARRIER();
    int g1o = r8 * G1_STRIDE + c7 * 16, g1s = WS_G1 + h * 8 * G1_STRIDE, g1q = g1s + 3 * rq_ * 128;
    SPIN(g1s); SPIN(g1q); PIN(g1o);
    if (PRETOUCH & 1) {
#pragma unroll
      for (int i = 0; i < 3; ++i) g[i] = __builtin_amdgcn_raw_buffer_load_b128(rws, g1o + i * 128, g1q, AUX_SC1);
      asm volatile("" :: "v"(g[0]), "v"(g[1]), "v"(g[2]));
    }
    BARRIER();
    const float mw = ms[c7], M = max8(mw);
    const float L = sum8(ls[c7] * __builtin_amdgcn_exp2f(mw - M));
    __builtin_amdgcn_raw_buffer_store_b128(v4u{asu(M), tag, asu(L), tag}, rws, lane < 8 ? lane * 16 : OFF, g1s + s * G1_STRIDE + 12 * 128, AUX_SC1);
    SB();
    __builtin_amdgcn_s_sleep(MDLY);
    _Pragma("nounroll") for (unsigned spin = 0; spin < SPIN_CAP; ++spin) {
      g[3] = __builtin_amdgcn_raw_buffer_load_b128(rws, g1o + 12 * 128, g1s, AUX_SC1);
      if (none(miss(g[3], tag))) break;
      asm volatile("" ::: "memory");
    }
    const float Ms = asf(g[3][0]), Mg = max_hi(Ms);
    const float Lg = sum_hi(__builtin_amdgcn_exp2f(Ms - Mg) * asf(g[3][2]));
    if (lane == 0) ctl[1] = asu(__builtin_amdgcn_exp2f(M - Mg) / Lg);
    LDSP unsigned* ow0 = os + 8 * c7 + (lane >> 4); LDSP unsigned* ow1 = os + 8 * c7 + 4 + (lane >> 5);
    PIN(ow0); PIN(ow1);
    STAMP(6);
    BARRIER();
    BARRIER();
    STAMP(8);
    BARRIER();
    STAMP(9);
    if (H1DLY) __builtin_amdgcn_s_sleep(H1DLY);
#define FIN1(d) do { STAMP(10); \
    float v0 = asf(d[0][0]), v1 = asf(d[0][2]), v2 = asf(d[1][0]), v3 = asf(d[1][2]), v4 = asf(d[2][0]), v5 = asf(d[2][2]); \
    swap32(v0, v2); v0 += v2; swap32(v1, v3); v1 += v3; swap32(v4, v5); v4 += v5; \
    swap16(v0, v1); v0 += v1; v5 = v4; swap16(v4, v5); v4 += v5; \
    v0 += dpp<ROR8>(v0); v4 += dpp<ROR8>(v4); \
    *ow0 = pk_bf16(v0, v0); *ow1 = pk_bf16(v4, v4); BARRIER(); } while (0)
#define LD1(d) _Pragma("unroll") for (int i = 0; i < 3; ++i) d[i] = __builtin_amdgcn_raw_buffer_load_b128(rws, g1o + i * 128, g1q, AUX_SC1); asm volatile("" ::: "memory")
    if (P1 == 0) {
      _Pragma("nounroll") for (unsigned spin = 0; spin < SPIN_CAP; ++spin) {
        LD1(g);
        if (none(miss(g[0], tag) | miss(g[1], tag) | miss(g[2], tag))) break;
        if (S1) __builtin_amdgcn_s_sleep(S1);
      }
    } else {
      v4u g2[3];
      LD1(g);
      __builtin_amdgcn_s_sleep(PSL);
      _Pragma("nounroll") for (unsigned spin = 0; spin < SPIN_CAP; ++spin) {
        LD1(g2); SB();
        if (none(miss(g[0], tag) | miss(g[1], tag) | miss(g[2], tag))) break;
        LD1(g); SB();
        if (none(miss(g2[0], tag) | miss(g2[1], tag) | miss(g2[2], tag))) { FIN1(g2); FINISH(0); }
      }
    }
    FIN1(g);
    FINISH(0);
  }
  {
    const int w = wave - 8;
    unsigned kvo = (unsigned)(h * SK + (SK - W) + (w * 8 + s) * 8) * (DH * 2);
    int lo = r8 * (DH * 2) + c7 * 16;
    PIN(lo); SPIN(kvo);
    ARG3(kc, 1, vc, 2, ws, 6);
    const rsrc_t rk = mk_rsrc((const char*)kc + kvo, (7 * 512 + 8) * DH * 2), rv = mk_rsrc((const char*)vc + kvo, (7 * 512 + 8) * DH * 2);
    v4u kv[48];
#define ISSUE(n) kv[n] = __builtin_amdgcn_raw_buffer_load_b128((n) < 24 ? rk : rv, lo + ((n) % 3) * 128, (((n) % 24) / 3) * 512 * DH * 2, AUX_NT)
    constexpr int WMAX = (WINV > WINE ? WINV : WINE);
    constexpr int N_SC = (N_QB + 7 > 23 + WIN) ? N_QB + 7 : 23 + WIN;
#define WOF(n) (((n) >= NE ? WINE : (n) > N_SC + 2 ? WINV : WIN) + ((XTRA && ((n) == N_QB || (n) == N_SC + 2)) ? XTRA : 0))
#define STEP(n) do { SB(); if ((n) % BURST == 0) asm volatile("s_waitcnt vmcnt(%0)" :: "n"(WOF(n) - BURST) : "memory"); ISSUE(n); SB(); } while (0)
#pragma unroll
    for (int n = 0; n < WIN0; ++n) ISSUE(n);
    STAMP(1);
    BARRIER();
#pragma unroll
    for (int n = WIN0; n <= N_QB; ++n) STEP(n);
    STAMP(3);
    BARRIER();
    STAMP(4);
    v4u qv[3];
#pragma unroll
    for (int t = 0; t < 3; ++t) qv[t] = *(const LDSP v4u*)(qs + 32 * t + 4 * c7);
    const unsigned tag = ctl[0];
    constexpr float C2 = 0.07216878364870322f * 1.4426950408889634f;
    float sc[NG];
#pragma unroll
    for (int n = N_QB; n <= N_SC; ++n) {
      if (n > N_QB) STEP(n);
#pragma unroll
      for (int j = 0; j < NG; ++j) {
        if (n == ((N_QB + j > 3 * j + 2 + WIN) ? N_QB + j : 3 * j + 2 + WIN)) {
          float a = 0.f;
#pragma unroll
          for (int t = 0; t < 3; ++t)
#pragma unroll
            for (int k = 0; k < 4; ++k) a = dot2(kv[3 * j + t][k], qv[t][k], a);
          sc[j] = sum8(a) * C2;
          PIN(sc[j]);
        }
      }
    }
    float m = sc[0];
#pragma unroll
    for (int j = 1; j < NG; ++j) m = fmaxf(m, sc[j]);
    m = max_hi(m);
    STEP(N_SC + 1);
    float l = 0.f;
#pragma unroll
    for (int j = 0; j < NG; ++j) { sc[j] = __builtin_amdgcn_exp2f(sc[j] - m); l += sc[j]; }
    l = sum_hi(l);
    if (lane == 0) { ms[w] = m; ls[w] = l; }
    STEP(N_SC + 2);
    STAMP(5);
    BARRIER();
    const float wt = __builtin_amdgcn_exp2f(m - max8(ms[c7]));
    MAKE_MSK();
    f4 acc[3] = {{0.f, 0.f, 0.f, 0.f}, {0.f, 0.f, 0.f, 0.f}, {0.f, 0.f, 0.f, 0.f}};
    v4u pa;
#define PV(n) do { if ((n) % 3 == 0) { const float p = sc[((n) - 24) / 3] * wt; pa = msk & pk_bf16(p, p); } acc[(n) % 3] = mfma(pa, kv[n], acc[(n) % 3]); } while (0)
#pragma unroll
    for (int n = 24; n <= N_SC + 2 - WMAX; ++n) PV(n);
#pragma unroll
    for (int n = N_SC + 3; n < 48; ++n) { STEP(n); if (n == N_WO) BARRIER(); if (n - WMAX >= 24) PV(n - WMAX); }
    STAMP(6);
    LDSP float* pp = P + (2 * w + (lane >> 5)) * PSTR + 8 * c7 + 4 * ((lane >> 4) & 1);
    const bool pvalid = (lane >> 5) == ((lane >> 3) & 1);
    const LDSP float* rp = P + 2 * r8 * PSTR + 32 * w + 4 * c7;
    const rsrc_t rws = mk_rsrc((void*)ws, WS_END);
    int so = (lane & 8) ? OFF : (lane >> 4) * 64 + c7 * 8;
    int ss = WS_G1 + (h * 8 + s) * G1_STRIDE + w * 256;
    float f = asf(ctl[1]);
    v2u sv; sv[1] = tag;
    PIN(so); SPIN(ss); PIN(f); PIN(pp); PIN(rp); PIN(sv);
#pragma unroll
    for (int n = 48 - WMAX; n < 45; ++n) PV(n);
#pragma unroll
    for (int n = 45; n < 48; ++n) { if (n >= 48 - WMAX) PV(n); if (pvalid) *(LDSP f4*)(pp + 64 * (n - 45)) = acc[n - 45]; SB(); }
    STAMP(7);
    BARRIER();
    STAMP(8);
    if (w < 6) {
      const f4 u = *(const LDSP f4*)rp + *(const LDSP f4*)(rp + PSTR);
      float a = u[0], b2 = u[1], c = u[2], e = u[3];
      swap32(a, c); a += c;
      swap32(b2, e); b2 += e;
      swap16(a, b2); a += b2;
      a += dpp<ROR8>(a);
      sv[0] = asu(a * f);
      __builtin_amdgcn_raw_buffer_store_b64(sv, rws, so, ss, AUX_SC1);
    }
    STAMP(9);
    BARRIER();
    FINISH(1);
  }
}

extern "C" void kernel_launch(void* const* d_in, const int* in_sizes, int n_in, void* d_out, int out_size, void* d_ws, size_t ws_size, hipStream_t stream) {
  fused_decode<<<256, NW * 64, 0, stream>>>((const u16*)d_in[0], (const u16*)d_in[1], (const u16*)d_in[2], (const u16*)d_in[3], (const u16*)d_in[4], (u16*)d_out, (unsigned char*)d_ws,
                                            (u64*)((char*)d_ws + (64 << 20)));
}
```

```cpp
#include <hip/hip_runtime.h>
#include <hip/hip_bf16.h>
#include <stdint.h>

typedef unsigned short u16;
typedef unsigned long long u64;
typedef unsigned v4u __attribute__((ext_vector_type(4)));
typedef unsigned v2u __attribute__((ext_vector_type(2)));
typedef float f4 __attribute__((ext_vector_type(4)));
typedef __bf16 bf16x2 __attribute__((ext_vector_type(2)));
typedef __bf16 bf16x8 __attribute__((ext_vector_type(8)));
typedef __amdgpu_buffer_rsrc_t rsrc_t;
#define LDSP __attribute__((address_space(3)))

constexpr int D = 640, H = 32, SK = 32768, DH = 192, W = 4096;
constexpr int NW = 16, NG = 8;
constexpr int WIN = 3;
constexpr int WIN0 = 3;
constexpr int N_QB = 20;
constexpr int QDLY = 70, MDLY = 70, H1DLY = 6, H2DLY = 12;
constexpr int CODE_BYTES = 8832;
constexpr unsigned SPIN_CAP = 1u << 20;
constexpr int AUX_NT = 2, AUX_SC1 = 16;
constexpr int OFF = 0x7ffffff0;

constexpr int WS_EPOCH = 0;
constexpr int WS_SD = 1024;
constexpr int WS_GQ = 4096;
constexpr int GQ_STRIDE = 10 * 512;
constexpr int WS_ML = WS_GQ + 32 * GQ_STRIDE;
constexpr int WS_PR = 224 << 10;
constexpr int WS_G1 = 256 << 10;
constexpr int G1_STRIDE = 13 * 128;
constexpr int WS_G2 = 768 << 10;
constexpr int WS_END = WS_G2 + 20 * 32768;
constexpr int PSTR = 208;

__device__ __forceinline__ rsrc_t mk_rsrc(const void* p, int bytes) { return __builtin_amdgcn_make_buffer_rsrc((void*)p, 0, bytes, 0x00020000); }
__device__ __forceinline__ u64 up2m(u64 p) { return (p + 0x1fffff) & ~0x1fffffull; }
__device__ __forceinline__ float asf(unsigned u) { return __builtin_bit_cast(float, u); }
__device__ __forceinline__ unsigned asu(float f) { return __builtin_bit_cast(unsigned, f); }
template <int CTRL> __device__ __forceinline__ float dpp(float x) { return asf(__builtin_amdgcn_mov_dpp(asu(x), CTRL, 0xf, 0xf, true)); }
constexpr int XOR1 = 0xB1, XOR2 = 0x4E, HMIR = 0x141, ROR8 = 0x128, ROR4 = 0x124;
__device__ __forceinline__ unsigned pk_bf16(float lo, float hi) { bf16x2 r = {(__bf16)lo, (__bf16)hi}; return __builtin_bit_cast(unsigned, r); }
__device__ __forceinline__ float dot2(unsigned a, unsigned b, float c) { return __builtin_amdgcn_fdot2_f32_bf16(__builtin_bit_cast(bf16x2, a), __builtin_bit_cast(bf16x2, b), c, false); }
__device__ __forceinline__ f4 mfma(v4u a, v4u b, f4 c) { return __builtin_amdgcn_mfma_f32_16x16x32_bf16(__builtin_bit_cast(bf16x8, a), __builtin_bit_cast(bf16x8, b), c, 0, 0, 0); }
__device__ __forceinline__ float sum8(float x) { x += dpp<XOR1>(x); x += dpp<XOR2>(x); x += dpp<HMIR>(x); return x; }
__device__ __forceinline__ float max8(float x) { x = fmaxf(x, dpp<XOR1>(x)); x = fmaxf(x, dpp<XOR2>(x)); x = fmaxf(x, dpp<HMIR>(x)); return x; }
__device__ __forceinline__ void swap32(float& a, float& b) { auto r = __builtin_amdgcn_permlane32_swap(asu(a), asu(b), false, false); a = asf(r[0]); b = asf(r[1]); }
__device__ __forceinline__ void swap16(float& a, float& b) { auto r = __builtin_amdgcn_permlane16_swap(asu(a), asu(b), false, false); a = asf(r[0]); b = asf(r[1]); }
__device__ __forceinline__ float sum_hi(float x) { x += dpp<ROR8>(x); float y = x; swap16(x, y); x += y; y = x; swap32(x, y); return x + y; }
__device__ __forceinline__ float max_hi(float x) { x = fmaxf(x, dpp<ROR8>(x)); float y = x; swap16(x, y); x = fmaxf(x, y); y = x; swap32(x, y); return fmaxf(x, y); }
__device__ __forceinline__ unsigned miss(v4u g, unsigned tag) { return (g[1] ^ tag) | (g[3] ^ tag); }
__device__ __forceinline__ bool none(unsigned x) { return __builtin_amdgcn_ballot_w64(x != 0) == 0; }
__device__ __forceinline__ unsigned tag_of(unsigned epoch) { return __builtin_amdgcn_readfirstlane(epoch) * 0x9E3779B1u + 0x7F4A7C15u; }
__device__ __forceinline__ v4u pos_mask(int c7) {
  v4u m;
#pragma unroll
  for (int d = 0; d < 4; ++d) m[d] = ((c7 >> 1) == d) ? ((c7 & 1) ? 0xffff0000u : 0x0000ffffu) : 0u;
  return m;
}
__device__ __forceinline__ void fold_tile(f4 d, float& o0, float& o1) {
  float a = d[0], b = d[2], c = d[1], e = d[3];
  swap32(a, b); swap32(c, e);
  o0 = a + dpp<ROR8>(b); o1 = c + dpp<ROR8>(e);
}

#define SB() __builtin_amdgcn_sched_barrier(0)
#define BARRIER() do { SB(); asm volatile("s_waitcnt lgkmcnt(0)\n\ts_barrier" ::: "memory"); SB(); } while (0)
#define PINV(x) asm volatile("" : "+v"(x))
#define PINS(x) asm volatile("" : "+s"(x))
#define POLL _Pragma("nounroll") for (unsigned spin = 0; spin < SPIN_CAP; ++spin)
#define AGAIN() asm volatile("" ::: "memory")

__global__ void __launch_bounds__(NW * 64, 4) fused_decode(const u16*, const u16*, const u16*, const u16*, const u16*, u16*, unsigned char*, const void*) {
  __shared__ __attribute__((aligned(16))) float lds[16 * PSTR + 96 + 64 + 8 + 8 + 4];
  LDSP float* P = (LDSP float*)lds;
  LDSP unsigned* qs = (LDSP unsigned*)(P + 16 * PSTR);
  LDSP unsigned* os = qs + 96;
  LDSP float* ms = P + 16 * PSTR + 96 + 64;
  LDSP float* ls = ms + 8;
  LDSP unsigned* ctl = (LDSP unsigned*)(ls + 8);
  asm volatile("s_load_dwordx16 s[64:79], %0, 0x0" :: "s"(__builtin_amdgcn_kernarg_segment_ptr())
               : "s64", "s65", "s66", "s67", "s68", "s69", "s70", "s71", "s72", "s73", "s74", "s75", "s76", "s77", "s78", "s79");
#define ARG3(p0, i0, p1, i1, p2, i2) u64 p0, p1, p2; \
  asm volatile("s_waitcnt lgkmcnt(0)\n\ts_mov_b64 %0, s[%c3:%c4]\n\ts_mov_b64 %1, s[%c5:%c6]\n\ts_mov_b64 %2, s[%c7:%c8]" : "=s"(p0), "=s"(p1), "=s"(p2) \
               : "i"(64 + 2 * i0), "i"(65 + 2 * i0), "i"(64 + 2 * i1), "i"(65 + 2 * i1), "i"(64 + 2 * i2), "i"(65 + 2 * i2))
  unsigned pf = 0;
  if (threadIdx.x < 128) {
    u64 pc; asm volatile("s_getpc_b64 %0" : "=s"(pc));
    pf = __builtin_amdgcn_raw_buffer_load_b32(mk_rsrc((void*)(pc & ~127ull), CODE_BYTES), threadIdx.x * 128 + 128, 0, 0);
  }
  SB();
  const int b = blockIdx.x, jb = b >> 3;
  const int h = (b & 7) * 4 + (jb >> 3), s = jb & 7;
  const int tid = threadIdx.x, wave = __builtin_amdgcn_readfirstlane(tid >> 6), lane = tid & 63, r8 = lane >> 3, c7 = lane & 7;
#define SIDE(c) (((sides >> (c)) & 1) << 13)

  if (wave >= 3 && wave < 8) {
    const int rq = s & 3, ch = s >> 2;
    ARG3(wo, 4, out, 5, ws0, 6);
    const u64 ws = up2m(ws0);
    const rsrc_t rws = mk_rsrc((void*)ws, WS_END), rout = mk_rsrc((void*)out, D * 2);
    unsigned ep = 0;
    if (b < 8) {
      __builtin_amdgcn_raw_buffer_store_b128(v4u{0u, 0u, 0u, 0u}, rws, lane < 16 ? r8 * 8192 + c7 * 16 : OFF, WS_PR + b * 1024 + wave * 128, AUX_SC1);
      ep = __builtin_amdgcn_raw_buffer_load_b32(rws, 0, WS_EPOCH, AUX_SC1);
    }
    BARRIER();
    const rsrc_t ro = mk_rsrc((const u16*)wo + (size_t)(h * DH) * D + 320 * ch + 64 * (wave - 3), DH * D * 2);
    v4u msk = pos_mask(c7);
    int wl = (8 * (r8 & 3) + (r8 >> 2)) * (D * 2) + c7 * 16;
    int wsf[3];
#pragma unroll
    for (int i = 0; i < 3; ++i) { const int n = 3 * rq + i; wsf[i] = (32 * (n >> 1) + 2 * (n & 1)) * (D * 2); PINS(wsf[i]); }
    const int yl = 4 * (wave - 3) + (lane >> 4), yp = h * 4 + rq;
    int ys = WS_G2 + (yp >> 6) * 16384 + (yp & 63) * 128;
    const int lc = (b & 7) * 5 + (jb >> 2), qd = jb & 3;
    const bool fin = wave == 4 && jb < 20;
    int fo = (lane >> 1) * 128 + (lane & 1) * 16;
    int oo = (lane & 30) ? OFF : 2 * (64 * (lc >> 2) + 8 * (2 * qd + (lane & 1)) + 4 * (lc & 1) + (lc & 2) + (lane >> 5));
    const LDSP unsigned* op = os + 8 * r8;
    PINV(fo); PINV(oo); PINV(wl); PINV(msk); PINV(op); PINS(ys);
    if (b < 8) {
      asm volatile("s_waitcnt vmcnt(0)" ::: "memory");
      const int first = (wave ^ b) & 1, o0 = WS_PR + b * 1024 + wave * 128 + first * 8192, o1 = o0 ^ 8192;
      u64 c0, c1, c2; unsigned unused;
      asm volatile("s_memtime %1\n\ts_waitcnt lgkmcnt(0)\n\ts_load_dword %0, %4, %5\n\ts_waitcnt lgkmcnt(0)\n\ts_memtime %2\n\ts_waitcnt lgkmcnt(0)\n\t"
                   "s_load_dword %0, %4, %6\n\ts_waitcnt lgkmcnt(0)\n\ts_memtime %3\n\ts_waitcnt lgkmcnt(0)"
                   : "=&s"(unused), "=&s"(c0), "=&s"(c1), "=&s"(c2) : "s"(ws), "s"(o0), "s"(o1) : "memory");
      const int hi = (__builtin_amdgcn_s_getreg(20 | (3 << 11)) >> 2) & 1;
      int d = max(-1000, min(1000, (int)(c1 - c0) - (int)(c2 - c1)));
      if (first ^ hi) d = -d;
      __builtin_amdgcn_raw_buffer_store_b64(v2u{(unsigned)(2 * d + hi), tag_of(ep)}, rws, lane ? OFF : b * 128 + wave * 8, WS_SD, AUX_SC1);
    }
    BARRIER();
    v4u yv; yv[1] = yv[3] = ctl[0];
    const unsigned tag = __builtin_amdgcn_readfirstlane(yv[1]);
    PINV(yv);
    if (wave == 7) {
      v2u sd;
      POLL {
        sd = __builtin_amdgcn_raw_buffer_load_b64(rws, r8 * 128 + max(c7, 3) * 8, WS_SD, AUX_SC1);
        if (none(sd[1] ^ tag)) break;
        AGAIN();
      }
      const float dd = sum_hi(sum8(c7 >= 3 ? (float)((int)sd[0] >> 1) : 0.f));
      const u64 bl = __builtin_amdgcn_ballot_w64(((sd[0] & 1) != 0) != (dd > 0.f));
      const unsigned sb = (bl & 1) | ((bl >> 7) & 2) | ((bl >> 14) & 4) | ((bl >> 21) & 8);
      if (lane == 0) ctl[3] = sb | ((sb ^ 15u) << 4);
    }
    BARRIER();
    const unsigned sides = __builtin_amdgcn_readfirstlane(ctl[3]);
    int fs[4];
#pragma unroll
    for (int i = 0; i < 4; ++i) { fs[i] = WS_G2 + (lc % 20) * 32768 + (i >> 1) * 16384 + SIDE(b & 7) + (i & 1) * 4096 + qd * 32; PINS(fs[i]); }
    int yo = (lane & 8) ? OFF : yl * 32768 + SIDE(4 * ch + yl / 5) + c7 * 16;
    PINV(yo);
    v4u g[4];
    if (fin) {
#pragma unroll
      for (int i = 0; i < 4; ++i) g[i] = __builtin_amdgcn_raw_buffer_load_b128(rws, fo, fs[i], AUX_SC1);
      asm volatile("" :: "v"(g[0]), "v"(g[1]), "v"(g[2]), "v"(g[3]));
    }
    BARRIER();
    v4u wr[6];
#pragma unroll
    for (int j = 0; j < 6; ++j) wr[j] = __builtin_amdgcn_raw_buffer_load_b128(ro, wl, wsf[j >> 1] + (j & 1) * 4 * (D * 2), AUX_NT);
    BARRIER();
    BARRIER();
    asm volatile("" :: "v"(wr[0]), "v"(wr[1]), "v"(wr[2]), "v"(wr[3]), "v"(wr[4]), "v"(wr[5]));
    BARRIER();
    const v4u oa = *(const LDSP v4u*)op;
    const v2u ob = *(const LDSP v2u*)(op + 4);
    f4 y = {0.f, 0.f, 0.f, 0.f};
#pragma unroll
    for (int j = 0; j < 6; ++j) y = mfma(msk & (j < 4 ? oa[j] : ob[j - 4]), wr[j], y);
    float y0, y1; fold_tile(y, y0, y1);
    yv[0] = asu(y0); yv[2] = asu(y1);
    __builtin_amdgcn_raw_buffer_store_b128(yv, rws, yo, ys, AUX_SC1);
    if (fin) {
      __builtin_amdgcn_s_sleep(H2DLY);
      unsigned bad = 1;
      POLL {
        if (bad) {
#pragma unroll
          for (int i = 0; i < 4; ++i) g[i] = __builtin_amdgcn_raw_buffer_load_b128(rws, fo, fs[i], AUX_SC1);
          bad = miss(g[0], tag) | miss(g[1], tag) | miss(g[2], tag) | miss(g[3], tag);
        }
        if (none(bad)) break;
        AGAIN();
      }
      float a0 = (asf(g[0][0]) + asf(g[1][0])) + (asf(g[2][0]) + asf(g[3][0]));
      float a1 = (asf(g[0][2]) + asf(g[1][2])) + (asf(g[2][2]) + asf(g[3][2]));
      swap32(a0, a1); a0 += a1;
      a1 = a0; swap16(a0, a1); a0 += a1;
      a0 += dpp<ROR8>(a0); a0 += dpp<ROR4>(a0); a0 += dpp<XOR2>(a0);
      __builtin_amdgcn_raw_buffer_store_b16((u16)pk_bf16(a0, a0), rout, oo, 0, 0);
    }
    __builtin_amdgcn_endpgm();
  }

  if (wave < 3) {
    const int u = 3 * s + wave;
    int wl = r8 * 10 * (H * DH * 2) + c7 * 16, xl = 20 * r8 + (u & 7) * 160;
    unsigned us = (u & 7) * 80 * (H * DH * 2) + (u >> 3) * 128 + h * (DH * 2);
    PINV(wl); PINV(xl); PINS(us);
    ARG3(wq, 3, x, 0, ws0, 6);
    const rsrc_t rq = mk_rsrc((const char*)wq + us, 79 * H * DH * 2 + 128), rx = mk_rsrc((void*)x, D * 2), rws = mk_rsrc((void*)up2m(ws0), WS_END);
    v4u wr[10], g[5];
#pragma unroll
    for (int j = 0; j < 10; ++j) wr[j] = __builtin_amdgcn_raw_buffer_load_b128(rq, wl, j * (H * DH * 2), AUX_NT);
    const v4u xa = __builtin_amdgcn_raw_buffer_load_b128(rx, xl, 0, 0);
    const unsigned xb = __builtin_amdgcn_raw_buffer_load_b32(rx, xl + 16, 0, 0);
    const unsigned ep = __builtin_amdgcn_raw_buffer_load_b32(rws, wave ? OFF : 0, WS_EPOCH, AUX_SC1);
    if (tid == 0) ctl[2] = 0;
    BARRIER();
    const v4u msk = pos_mask(c7);
    f4 acc[3];
    acc[0] = f4{0.f, 0.f, 0.f, 0.f};
#pragma unroll
    for (int j = 0; j < 10; ++j) {
      const unsigned xw = j < 8 ? xa[j >> 1] : xb;
      acc[0] = mfma(msk & __builtin_amdgcn_perm(xw, xw, (j & 1) ? 0x03020302u : 0x01000100u), wr[j], acc[0]);
    }
    asm volatile("" :: "v"(pf));
    if (wave) {
      *(LDSP f4*)(P + (wave - 1) * 256 + lane * 4) = acc[0];
      asm volatile("s_waitcnt lgkmcnt(0)" ::: "memory");
      if (lane == 0) __hip_atomic_fetch_add(ctl + 2, 1u, __ATOMIC_RELAXED, __HIP_MEMORY_SCOPE_WORKGROUP);
      if (wave == 2 || b != 0) __builtin_amdgcn_endpgm();
      BARRIER();
      const unsigned tag = __builtin_amdgcn_readfirstlane(ctl[0]);
      BARRIER();
      __builtin_amdgcn_s_sleep(MDLY);
      POLL {
        if (none(miss(__builtin_amdgcn_raw_buffer_load_b128(rws, (lane & 31) * 1024, WS_ML, AUX_SC1), tag))) break;
        __builtin_amdgcn_s_sleep(8);
        AGAIN();
      }
      __builtin_amdgcn_raw_buffer_store_b32(tag, rws, lane ? OFF : 0, WS_EPOCH, AUX_SC1);
      __builtin_amdgcn_endpgm();
    }
    while (__hip_atomic_load(ctl + 2, __ATOMIC_RELAXED, __HIP_MEMORY_SCOPE_WORKGROUP) != 2u) __builtin_amdgcn_s_sleep(1);
    AGAIN();
    acc[1] = *(const LDSP f4*)(P + lane * 4); acc[2] = *(const LDSP f4*)(P + 256 + lane * 4);
    const unsigned tag = tag_of(ep);
    int gqs = WS_GQ + h * GQ_STRIDE;
    PINS(gqs);
    const int rec = s + (s > 2) + (s > 5);
    const int to = (lane & 8) ? OFF : (lane >> 4) * 128 + c7 * 16;
    float q0, q1;
    if (s == 2 || s == 5) {
      fold_tile(s == 2 ? acc[2] : acc[1] + acc[2], q0, q1);
      __builtin_amdgcn_raw_buffer_store_b128(v4u{asu(q0), tag, asu(q1), tag}, rws, to, gqs + (rec + 1) * 512, AUX_SC1);
      fold_tile(s == 2 ? acc[0] + acc[1] : acc[0], q0, q1);
    } else fold_tile((acc[0] + acc[1]) + acc[2], q0, q1);
    __builtin_amdgcn_raw_buffer_store_b128(v4u{asu(q0), tag, asu(q1), tag}, rws, to, gqs + rec * 512, AUX_SC1);
    SB();
    __builtin_amdgcn_s_sleep(QDLY);
    POLL {
#pragma unroll
      for (int i = 0; i < 5; ++i) g[i] = __builtin_amdgcn_raw_buffer_load_b128(rws, lane * 16, gqs + i * 1024, AUX_SC1);
      if (none(miss(g[0], tag) | miss(g[1], tag) | miss(g[2], tag) | miss(g[3], tag) | miss(g[4], tag))) break;
      AGAIN();
    }
    {
      const bool hi = lane >= 32;
      float qa[3][2];
#pragma unroll
      for (int e = 0; e < 2; ++e) {
        qa[0][e] = asf(g[0][2 * e]) + (hi ? 0.f : asf(g[1][2 * e]));
        qa[1][e] = asf(g[2][2 * e]) + (hi ? asf(g[1][2 * e]) : asf(g[3][2 * e]));
        qa[2][e] = asf(g[4][2 * e]) + (hi ? asf(g[3][2 * e]) : 0.f);
      }
#pragma unroll
      for (int t = 0; t < 3; ++t) {
        float a0 = qa[t][0], a1 = qa[t][1], b0 = a0, b1 = a1;
        swap32(a0, b0); swap32(a1, b1);
        qs[32 * t + 4 * c7 + 2 * (r8 & 1) + ((r8 >> 1) & 1)] = pk_bf16(a0 + b0, a1 + b1);
      }
    }
    if (lane == 0) ctl[0] = tag;
    BARRIER();
    BARRIER();
    const unsigned sides = __builtin_amdgcn_readfirstlane(ctl[3]);
    int g1o = (r8 >> 2) * 16384 + (r8 & 3) * G1_STRIDE + c7 * 16, g1q = WS_G1 + (h & 15) * 32768 + SIDE(b & 7) + 3 * (s & 3) * 128;
    int mls = WS_ML + h * 1024;
    PINS(mls); PINS(g1q); PINV(g1o);
    const float mw = ms[c7], M = max8(mw);
    const float L = sum8(ls[c7] * __builtin_amdgcn_exp2f(mw - M));
    __builtin_amdgcn_raw_buffer_store_b128(v4u{asu(M), tag, asu(L), tag}, rws, lane < 8 ? lane * 16 : OFF, mls + s * 128, AUX_SC1);
    SB();
#pragma unroll
    for (int i = 0; i < 3; ++i) g[i] = __builtin_amdgcn_raw_buffer_load_b128(rws, g1o + i * 128, g1q, AUX_SC1);
    SB();
    __builtin_amdgcn_s_sleep(MDLY);
    asm volatile("" :: "v"(g[0]), "v"(g[1]), "v"(g[2]));
    POLL {
      g[3] = __builtin_amdgcn_raw_buffer_load_b128(rws, lane * 16, mls, AUX_SC1);
      if (none(miss(g[3], tag))) break;
      AGAIN();
    }
    const float Ms = asf(g[3][0]), Mg = max_hi(Ms);
    const float Lg = sum_hi(__builtin_amdgcn_exp2f(Ms - Mg) * asf(g[3][2]));
    if (lane == 0) ctl[1] = asu(__builtin_amdgcn_exp2f(M - Mg) / Lg);
    LDSP unsigned* ow0 = os + 8 * c7 + (lane >> 4); LDSP unsigned* ow1 = os + 8 * c7 + 4 + (lane >> 5);
    PINV(ow0); PINV(ow1);
    BARRIER();
    BARRIER();
    BARRIER();
    __builtin_amdgcn_s_sleep(H1DLY);
    POLL {
#pragma unroll
      for (int i = 0; i < 3; ++i) g[i] = __builtin_amdgcn_raw_buffer_load_b128(rws, g1o + i * 128, g1q, AUX_SC1);
      if (none(miss(g[0], tag) | miss(g[1], tag) | miss(g[2], tag))) break;
      AGAIN();
    }
    float v0 = asf(g[0][0]), v1 = asf(g[0][2]), v2 = asf(g[1][0]), v3 = asf(g[1][2]), v4 = asf(g[2][0]), v5 = asf(g[2][2]);
    swap32(v0, v2); v0 += v2;
    swap32(v1, v3); v1 += v3;
    swap32(v4, v5); v4 += v5;
    swap16(v0, v1); v0 += v1;
    v5 = v4; swap16(v4, v5); v4 += v5;
    v0 += dpp<ROR8>(v0); v4 += dpp<ROR8>(v4);
    *ow0 = pk_bf16(v0, v0); *ow1 = pk_bf16(v4, v4);
    BARRIER();
    __builtin_amdgcn_endpgm();
  }

  {
    const int w = wave - 8;
    unsigned kvo = (unsigned)(h * SK + (SK - W) + (w * 8 + ((s + w) & 7)) * 8) * (DH * 2);
    int lo = r8 * (DH * 2) + c7 * 16;
    PINV(lo); PINS(kvo);
    ARG3(kc, 1, vc, 2, ws0, 6);
    const rsrc_t rk = mk_rsrc((const char*)kc + kvo, (7 * 512 + 8) * DH * 2), rv = mk_rsrc((const char*)vc + kvo, (7 * 512 + 8) * DH * 2);
    v4u kv[48];
#define ISSUE(n) kv[n] = __builtin_amdgcn_raw_buffer_load_b128((n) < 24 ? rk : rv, lo + ((n) % 3) * 128, (((n) % 24) / 3) * 512 * DH * 2, AUX_NT)
#define STEP(n) do { SB(); asm volatile("s_waitcnt vmcnt(%0)" :: "n"(WIN - 1) : "memory"); ISSUE(n); SB(); } while (0)
#pragma unroll
    for (int n = 0; n < WIN0; ++n) ISSUE(n);
    BARRIER();
#pragma unroll
    for (int n = WIN0; n <= N_QB; ++n) STEP(n);
    BARRIER();
    v4u qv[3];
#pragma unroll
    for (int t = 0; t < 3; ++t) qv[t] = *(const LDSP v4u*)(qs + 32 * t + 4 * c7);
    const unsigned tag = ctl[0];
    constexpr float C2 = 0.07216878364870322f * 1.4426950408889634f;
    constexpr int N_SC = (N_QB + 7 > 23 + WIN) ? N_QB + 7 : 23 + WIN;
    float sc[NG];
#pragma unroll
    for (int n = N_QB; n <= N_SC; ++n) {
      if (n > N_QB) STEP(n);
#pragma unroll
      for (int j = 0; j < NG; ++j) {
        if (n == ((N_QB + j > 3 * j + 2 + WIN) ? N_QB + j : 3 * j + 2 + WIN)) {
          float a = 0.f;
#pragma unroll
          for (int t = 0; t < 3; ++t)
#pragma unroll
            for (int k = 0; k < 4; ++k) a = dot2(kv[3 * j + t][k], qv[t][k], a);
          sc[j] = sum8(a) * C2;
          PINV(sc[j]);
        }
      }
    }
    float m = sc[0];
#pragma unroll
    for (int j = 1; j < NG; ++j) m = fmaxf(m, sc[j]);
    m = max_hi(m);
    STEP(N_SC + 1);
    float l = 0.f;
#pragma unroll
    for (int j = 0; j < NG; ++j) { sc[j] = __builtin_amdgcn_exp2f(sc[j] - m); l += sc[j]; }
    l = sum_hi(l);
    if (lane == 0) { ms[w] = m; ls[w] = l; }
    STEP(N_SC + 2);
    BARRIER();
    const float wt = __builtin_amdgcn_exp2f(m - max8(ms[c7]));
    const v4u msk = pos_mask(c7);
    f4 acc[3] = {{0.f, 0.f, 0.f, 0.f}, {0.f, 0.f, 0.f, 0.f}, {0.f, 0.f, 0.f, 0.f}};
    v4u pa;
#define PV(n) do { if ((n) % 3 == 0) { const float p = sc[((n) - 24) / 3] * wt; pa = msk & pk_bf16(p, p); } acc[(n) % 3] = mfma(pa, kv[n], acc[(n) % 3]); } while (0)
#pragma unroll
    for (int n = 24; n <= N_SC + 2 - WIN; ++n) PV(n);
#pragma unroll
    for (int n = N_SC + 3; n < 48; ++n) { STEP(n); if (n == 47) BARRIER(); PV(n - WIN); }
    LDSP float* pp = P + (2 * w + (lane >> 5)) * PSTR + 8 * c7 + 4 * ((lane >> 4) & 1);
    const bool pvalid = (lane >> 5) == ((lane >> 3) & 1);
    const LDSP float* rp = P + 2 * r8 * PSTR + 32 * w + 4 * c7;
    const rsrc_t rws = mk_rsrc((void*)up2m(ws0), WS_END);
    const unsigned sides = __builtin_amdgcn_readfirstlane(ctl[3]);
    int so = (lane & 8) ? OFF : (lane >> 4) * 64 + c7 * 8;
    int ss = WS_G1 + (h & 15) * 32768 + SIDE(b & 7) + (s >> 2) * 16384 + (s & 3) * G1_STRIDE + w * 256;
    float f = asf(ctl[1]);
    v2u sv; sv[1] = tag;
    PINV(so); PINS(ss); PINV(f); PINV(pp); PINV(rp); PINV(sv);
#pragma unroll
    for (int n = 48 - WIN; n < 45; ++n) PV(n);
#pragma unroll
    for (int n = 45; n < 48; ++n) { if (n >= 48 - WIN) PV(n); if (pvalid) *(LDSP f4*)(pp + 64 * (n - 45)) = acc[n - 45]; SB(); }
    BARRIER();
    if (w < 6) {
      const f4 t = *(const LDSP f4*)rp + *(const LDSP f4*)(rp + PSTR);
      float t0 = t[0], t1 = t[1], t2 = t[2], t3 = t[3];
      swap32(t0, t2); t0 += t2;
      swap32(t1, t3); t1 += t3;
      swap16(t0, t1); t0 += t1;
      t0 += dpp<ROR8>(t0);
      sv[0] = asu(t0 * f);
      __builtin_amdgcn_raw_buffer_store_b64(sv, rws, so, ss, AUX_SC1);
    }
    BARRIER();
    __builtin_amdgcn_endpgm();
  }
}

extern "C" void kernel_launch(void* const* d_in, const int* in_sizes, int n_in, void* d_out, int out_size, void* d_ws, size_t ws_size, hipStream_t stream) {
  fused_decode<<<256, NW * 64, 0, stream>>>((const u16*)d_in[0], (const u16*)d_in[1], (const u16*)d_in[2], (const u16*)d_in[3], (const u16*)d_in[4],
                                            (u16*)d_out, (unsigned char*)d_ws, d_ws);
}
```

```cpp
#include <hip/hip_runtime.h>
#include <hip/hip_bf16.h>
#include <stdint.h>

typedef unsigned short u16;
typedef unsigned long long u64;
typedef unsigned v4u __attribute__((ext_vector_type(4)));
typedef unsigned v2u __attribute__((ext_vector_type(2)));
typedef float f4 __attribute__((ext_vector_type(4)));
typedef __bf16 bf16x2 __attribute__((ext_vector_type(2)));
typedef __bf16 bf16x8 __attribute__((ext_vector_type(8)));
typedef __amdgpu_buffer_rsrc_t rsrc_t;
#define LDSP __attribute__((address_space(3)))

constexpr int D = 640, H = 32, SK = 32768, DH = 192, W = 4096;
constexpr int NW = 16, NG = 8;
constexpr int WIN = 3;
constexpr int WIN0 = 3;
constexpr int N_QB = 20;
constexpr int QDLY = 70, MDLY = 70, H1DLY = 6, H2DLY = 12;
constexpr int CODE_BYTES = 8832;
constexpr unsigned SPIN_CAP = 1u << 20;
constexpr int AUX_NT = 2, AUX_SC1 = 16;
constexpr int OFF = 0x7ffffff0;

constexpr int WS_EPOCH = 0;
constexpr int WS_SD = 1024;
constexpr int WS_GQ = 4096;
constexpr int GQ_STRIDE = 10 * 512;
constexpr int WS_ML = WS_GQ + 32 * GQ_STRIDE;
constexpr int WS_PR = 224 << 10;
constexpr int WS_G1 = 256 << 10;
constexpr int G1_STRIDE = 13 * 128;
constexpr int WS_G2 = 768 << 10;
constexpr int WS_END = WS_G2 + 20 * 32768;
constexpr int PSTR = 208;

__device__ __forceinline__ rsrc_t mk_rsrc(const void* p, int bytes) { return __builtin_amdgcn_make_buffer_rsrc((void*)p, 0, bytes, 0x00020000); }
__device__ __forceinline__ u64 up2m(u64 p) { return (p + 0x1fffff) & ~0x1fffffull; }
__device__ __forceinline__ float asf(unsigned u) { return __builtin_bit_cast(float, u); }
__device__ __forceinline__ unsigned asu(float f) { return __builtin_bit_cast(unsigned, f); }
template <int CTRL> __device__ __forceinline__ float dpp(float x) { return asf(__builtin_amdgcn_mov_dpp(asu(x), CTRL, 0xf, 0xf, true)); }
constexpr int XOR1 = 0xB1, XOR2 = 0x4E, HMIR = 0x141, ROR8 = 0x128, ROR4 = 0x124;
__device__ __forceinline__ unsigned pk_bf16(float lo, float hi) { bf16x2 r = {(__bf16)lo, (__bf16)hi}; return __builtin_bit_cast(unsigned, r); }
__device__ __forceinline__ float dot2(unsigned a, unsigned b, float c) { return __builtin_amdgcn_fdot2_f32_bf16(__builtin_bit_cast(bf16x2, a), __builtin_bit_cast(bf16x2, b), c, false); }
__device__ __forceinline__ f4 mfma(v4u a, v4u b, f4 c) { return __builtin_amdgcn_mfma_f32_16x16x32_bf16(__builtin_bit_cast(bf16x8, a), __builtin_bit_cast(bf16x8, b), c, 0, 0, 0); }
__device__ __forceinline__ float sum8(float x) { x += dpp<XOR1>(x); x += dpp<XOR2>(x); x += dpp<HMIR>(x); return x; }
__device__ __forceinline__ float max8(float x) { x = fmaxf(x, dpp<XOR1>(x)); x = fmaxf(x, dpp<XOR2>(x)); x = fmaxf(x, dpp<HMIR>(x)); return x; }
__device__ __forceinline__ void swap32(float& a, float& b) { auto r = __builtin_amdgcn_permlane32_swap(asu(a), asu(b), false, false); a = asf(r[0]); b = asf(r[1]); }
__device__ __forceinline__ void swap16(float& a, float& b) { auto r = __builtin_amdgcn_permlane16_swap(asu(a), asu(b), false, false); a = asf(r[0]); b = asf(r[1]); }
__device__ __forceinline__ float sum_hi(float x) { x += dpp<ROR8>(x); float y = x; swap16(x, y); x += y; y = x; swap32(x, y); return x + y; }
__device__ __forceinline__ float max_hi(float x) { x = fmaxf(x, dpp<ROR8>(x)); float y = x; swap16(x, y); x = fmaxf(x, y); y = x; swap32(x, y); return fmaxf(x, y); }
__device__ __forceinline__ unsigned miss(v4u g, unsigned tag) { return (g[1] ^ tag) | (g[3] ^ tag); }
__device__ __forceinline__ bool none(unsigned x) { return __builtin_amdgcn_ballot_w64(x != 0) == 0; }
__device__ __forceinline__ unsigned tag_of(unsigned epoch) { return __builtin_amdgcn_readfirstlane(epoch) * 0x9E3779B1u + 0x7F4A7C15u; }
__device__ __forceinline__ v4u pos_mask(int c7) {
  v4u m;
#pragma unroll
  for (int d = 0; d < 4; ++d) m[d] = ((c7 >> 1) == d) ? ((c7 & 1) ? 0xffff0000u : 0x0000ffffu) : 0u;
  return m;
}
__device__ __forceinline__ void fold_tile(f4 d, float& o0, float& o1) {
  float a = d[0], b = d[2], c = d[1], e = d[3];
  swap32(a, b); swap32(c, e);
  o0 = a + dpp<ROR8>(b); o1 = c + dpp<ROR8>(e);
}

#define SB() __builtin_amdgcn_sched_barrier(0)
#define BARRIER() do { SB(); asm volatile("s_waitcnt lgkmcnt(0)\n\ts_barrier" ::: "memory"); SB(); } while (0)
#define PINV(x) asm volatile("" : "+v"(x))
#define PINS(x) asm volatile("" : "+s"(x))
#define POLL _Pragma("nounroll") for (unsigned spin = 0; spin < SPIN_CAP; ++spin)
#define AGAIN() asm volatile("" ::: "memory")

__global__ void __launch_bounds__(NW * 64, 4) fused_decode(const u16*, const u16*, const u16*, const u16*, const u16*, u16*, unsigned char*, const void*) {
  __shared__ __attribute__((aligned(16))) float lds[16 * PSTR + 96 + 64 + 8 + 8 + 4];
  LDSP float* P = (LDSP float*)lds;
  LDSP unsigned* qs = (LDSP unsigned*)(P + 16 * PSTR);
  LDSP unsigned* os = qs + 96;
  LDSP float* ms = P + 16 * PSTR + 96 + 64;
  LDSP float* ls = ms + 8;
  LDSP unsigned* ctl = (LDSP unsigned*)(ls + 8);
  u64 t00; asm volatile("s_memrealtime %0\n\ts_waitcnt lgkmcnt(0)" : "=s"(t00) :: "memory"); SB();
  asm volatile("s_load_dwordx16 s[64:79], %0, 0x0" :: "s"(__builtin_amdgcn_kernarg_segment_ptr())
               : "s64", "s65", "s66", "s67", "s68", "s69", "s70", "s71", "s72", "s73", "s74", "s75", "s76", "s77", "s78", "s79");
  u64 tb; SB(); asm volatile("s_waitcnt lgkmcnt(0)"); asm volatile("s_memrealtime %0\n\ts_waitcnt lgkmcnt(0)" : "=s"(tb) :: "memory"); SB();
#define ARG3(p0, i0, p1, i1, p2, i2) u64 p0, p1, p2; \
  asm volatile("s_waitcnt lgkmcnt(0)\n\ts_mov_b64 %0, s[%c3:%c4]\n\ts_mov_b64 %1, s[%c5:%c6]\n\ts_mov_b64 %2, s[%c7:%c8]" : "=s"(p0), "=s"(p1), "=s"(p2) \
               : "i"(64 + 2 * i0), "i"(65 + 2 * i0), "i"(64 + 2 * i1), "i"(65 + 2 * i1), "i"(64 + 2 * i2), "i"(65 + 2 * i2))
  unsigned pf = 0;
  if (threadIdx.x < 128) {
    u64 pc; asm volatile("s_getpc_b64 %0" : "=s"(pc));
    pf = __builtin_amdgcn_raw_buffer_load_b32(mk_rsrc((void*)(pc & ~127ull), CODE_BYTES), threadIdx.x * 128 + 128, 0, 0);
  }
  SB();
  const int b = blockIdx.x, jb = b >> 3;
  const int h = (b & 7) * 4 + (jb >> 3), s = jb & 7;
  const int tid = threadIdx.x, wave = __builtin_amdgcn_readfirstlane(tid >> 6), lane = tid & 63, r8 = lane >> 3, c7 = lane & 7;
#define SIDE(c) (((sides >> (c)) & 1) << 13)

  if (wave >= 3 && wave < 8) {
    const int rq = s & 3, ch = s >> 2;
    ARG3(wo, 4, out, 5, ws0, 6);
    const u64 ws = up2m(ws0);
    const rsrc_t rws = mk_rsrc((void*)ws, WS_END), rout = mk_rsrc((void*)out, D * 2);
    unsigned ep = 0;
    if (b < 8) {
      __builtin_amdgcn_raw_buffer_store_b128(v4u{0u, 0u, 0u, 0u}, rws, lane < 16 ? r8 * 8192 + c7 * 16 : OFF, WS_PR + b * 1024 + wave * 128, AUX_SC1);
      ep = __builtin_amdgcn_raw_buffer_load_b32(rws, 0, WS_EPOCH, AUX_SC1);
    }
    BARRIER();
    const rsrc_t ro = mk_rsrc((const u16*)wo + (size_t)(h * DH) * D + 320 * ch + 64 * (wave - 3), DH * D * 2);
    v4u msk = pos_mask(c7);
    int wl = (8 * (r8 & 3) + (r8 >> 2)) * (D * 2) + c7 * 16;
    int wsf[3];
#pragma unroll
    for (int i = 0; i < 3; ++i) { const int n = 3 * rq + i; wsf[i] = (32 * (n >> 1) + 2 * (n & 1)) * (D * 2); PINS(wsf[i]); }
    const int yl = 4 * (wave - 3) + (lane >> 4), yp = h * 4 + rq;
    int ys = WS_G2 + (yp >> 6) * 16384 + (yp & 63) * 128;
    const int lc = (b & 7) * 5 + (jb >> 2), qd = jb & 3;
    const bool fin = wave == 4 && jb < 20;
    int fo = (lane >> 1) * 128 + (lane & 1) * 16;
    int oo = (lane & 30) ? OFF : 2 * (64 * (lc >> 2) + 8 * (2 * qd + (lane & 1)) + 4 * (lc & 1) + (lc & 2) + (lane >> 5));
    const LDSP unsigned* op = os + 8 * r8;
    PINV(fo); PINV(oo); PINV(wl); PINV(msk); PINV(op); PINS(ys);
    if (b < 8) {
      asm volatile("s_waitcnt vmcnt(0)" ::: "memory");
      const int first = (wave ^ b) & 1, o0 = WS_PR + b * 1024 + wave * 128 + first * 8192, o1 = o0 ^ 8192;
      u64 c0, c1, c2; unsigned unused;
      asm volatile("s_memtime %1\n\ts_waitcnt lgkmcnt(0)\n\ts_load_dword %0, %4, %5\n\ts_waitcnt lgkmcnt(0)\n\ts_memtime %2\n\ts_waitcnt lgkmcnt(0)\n\t"
                   "s_load_dword %0, %4, %6\n\ts_waitcnt lgkmcnt(0)\n\ts_memtime %3\n\ts_waitcnt lgkmcnt(0)"
                   : "=&s"(unused), "=&s"(c0), "=&s"(c1), "=&s"(c2) : "s"(ws), "s"(o0), "s"(o1) : "memory");
      const int hi = (__builtin_amdgcn_s_getreg(20 | (3 << 11)) >> 2) & 1;
      int d = max(-1000, min(1000, (int)(c1 - c0) - (int)(c2 - c1)));
      if (first ^ hi) d = -d;
      __builtin_amdgcn_raw_buffer_store_b64(v2u{(unsigned)(2 * d + hi), tag_of(ep)}, rws, lane ? OFF : b * 128 + wave * 8, WS_SD, AUX_SC1);
    }
    BARRIER();
    v4u yv; yv[1] = yv[3] = ctl[0];
    const unsigned tag = __builtin_amdgcn_readfirstlane(yv[1]);
    PINV(yv);
    if (wave == 7) {
      v2u sd;
      POLL {
        sd = __builtin_amdgcn_raw_buffer_load_b64(rws, r8 * 128 + max(c7, 3) * 8, WS_SD, AUX_SC1);
        if (none(sd[1] ^ tag)) break;
        AGAIN();
      }
      const float dd = sum_hi(sum8(c7 >= 3 ? (float)((int)sd[0] >> 1) : 0.f));
      const u64 bl = __builtin_amdgcn_ballot_w64(((sd[0] & 1) != 0) != (dd > 0.f));
      const unsigned sb = (bl & 1) | ((bl >> 7) & 2) | ((bl >> 14) & 4) | ((bl >> 21) & 8);
      if (lane == 0) ctl[3] = sb | ((sb ^ 15u) << 4);
    }
    BARRIER();
    const unsigned sides = __builtin_amdgcn_readfirstlane(ctl[3]);
    int fs[4];
#pragma unroll
    for (int i = 0; i < 4; ++i) { fs[i] = WS_G2 + (lc % 20) * 32768 + (i >> 1) * 16384 + SIDE(b & 7) + (i & 1) * 4096 + qd * 32; PINS(fs[i]); }
    int yo = (lane & 8) ? OFF : yl * 32768 + SIDE(4 * ch + yl / 5) + c7 * 16;
    PINV(yo);
    v4u g[4];
    if (fin) {
#pragma unroll
      for (int i = 0; i < 4; ++i) g[i] = __builtin_amdgcn_raw_buffer_load_b128(rws, fo, fs[i], AUX_SC1);
      asm volatile("" :: "v"(g[0]), "v"(g[1]), "v"(g[2]), "v"(g[3]));
    }
    BARRIER();
    v4u wr[6];
#pragma unroll
    for (int j = 0; j < 6; ++j) wr[j] = __builtin_amdgcn_raw_buffer_load_b128(ro, wl, wsf[j >> 1] + (j & 1) * 4 * (D * 2), AUX_NT);
    BARRIER();
    BARRIER();
    asm volatile("" :: "v"(wr[0]), "v"(wr[1]), "v"(wr[2]), "v"(wr[3]), "v"(wr[4]), "v"(wr[5]));
    BARRIER();
    const v4u oa = *(const LDSP v4u*)op;
    const v2u ob = *(const LDSP v2u*)(op + 4);
    f4 y = {0.f, 0.f, 0.f, 0.f};
#pragma unroll
    for (int j = 0; j < 6; ++j) y = mfma(msk & (j < 4 ? oa[j] : ob[j - 4]), wr[j], y);
    float y0, y1; fold_tile(y, y0, y1);
    yv[0] = asu(y0); yv[2] = asu(y1);
    __builtin_amdgcn_raw_buffer_store_b128(yv, rws, yo, ys, AUX_SC1);
    if (fin) {
      __builtin_amdgcn_s_sleep(H2DLY);
      unsigned bad = 1;
      POLL {
        if (bad) {
#pragma unroll
          for (int i = 0; i < 4; ++i) g[i] = __builtin_amdgcn_raw_buffer_load_b128(rws, fo, fs[i], AUX_SC1);
          bad = miss(g[0], tag) | miss(g[1], tag) | miss(g[2], tag) | miss(g[3], tag);
        }
        if (none(bad)) break;
        AGAIN();
      }
      float a0 = (asf(g[0][0]) + asf(g[1][0])) + (asf(g[2][0]) + asf(g[3][0]));
      float a1 = (asf(g[0][2]) + asf(g[1][2])) + (asf(g[2][2]) + asf(g[3][2]));
      swap32(a0, a1); a0 += a1;
      a1 = a0; swap16(a0, a1); a0 += a1;
      a0 += dpp<ROR8>(a0); a0 += dpp<ROR4>(a0); a0 += dpp<XOR2>(a0);
      __builtin_amdgcn_raw_buffer_store_b16((u16)pk_bf16(a0, a0), rout, oo, 0, 0);
    }
    __builtin_amdgcn_endpgm();
  }

  if (wave < 3) {
    const int u = 3 * s + wave;
    int wl = r8 * 10 * (H * DH * 2) + c7 * 16, xl = 20 * r8 + (u & 7) * 160;
    unsigned us = (u & 7) * 80 * (H * DH * 2) + (u >> 3) * 128 + h * (DH * 2);
    PINV(wl); PINV(xl); PINS(us);
    ARG3(wq, 3, x, 0, ws0, 6);
    const rsrc_t rq = mk_rsrc((const char*)wq + us, 79 * H * DH * 2 + 128), rx = mk_rsrc((void*)x, D * 2), rws = mk_rsrc((void*)up2m(ws0), WS_END);
    v4u wr[10], g[5];
#pragma unroll
    for (int j = 0; j < 10; ++j) wr[j] = __builtin_amdgcn_raw_buffer_load_b128(rq, wl, j * (H * DH * 2), AUX_NT);
    const v4u xa = __builtin_amdgcn_raw_buffer_load_b128(rx, xl, 0, 0);
    const unsigned xb = __builtin_amdgcn_raw_buffer_load_b32(rx, xl + 16, 0, 0);
    const unsigned ep = __builtin_amdgcn_raw_buffer_load_b32(rws, wave ? OFF : 0, WS_EPOCH, AUX_SC1);
    if (tid == 0) ctl[2] = 0;
    BARRIER();
    const v4u msk = pos_mask(c7);
    f4 acc[3];
    acc[0] = f4{0.f, 0.f, 0.f, 0.f};
#pragma unroll
    for (int j = 0; j < 10; ++j) {
      const unsigned xw = j < 8 ? xa[j >> 1] : xb;
      acc[0] = mfma(msk & __builtin_amdgcn_perm(xw, xw, (j & 1) ? 0x03020302u : 0x01000100u), wr[j], acc[0]);
    }
    asm volatile("" :: "v"(pf));
    if (wave) {
      *(LDSP f4*)(P + (wave - 1) * 256 + lane * 4) = acc[0];
      asm volatile("s_waitcnt lgkmcnt(0)" ::: "memory");
      if (lane == 0) __hip_atomic_fetch_add(ctl + 2, 1u, __ATOMIC_RELAXED, __HIP_MEMORY_SCOPE_WORKGROUP);
      if (wave == 2 || b != 0) __builtin_amdgcn_endpgm();
      BARRIER();
      const unsigned tag = __builtin_amdgcn_readfirstlane(ctl[0]);
      BARRIER();
      __builtin_amdgcn_s_sleep(MDLY);
      POLL {
        if (none(miss(__builtin_amdgcn_raw_buffer_load_b128(rws, (lane & 31) * 1024, WS_ML, AUX_SC1), tag))) break;
        __builtin_amdgcn_s_sleep(8);
        AGAIN();
      }
      __builtin_amdgcn_raw_buffer_store_b32(tag, rws, lane ? OFF : 0, WS_EPOCH, AUX_SC1);
      __builtin_amdgcn_endpgm();
    }
    while (__hip_atomic_load(ctl + 2, __ATOMIC_RELAXED, __HIP_MEMORY_SCOPE_WORKGROUP) != 2u) __builtin_amdgcn_s_sleep(1);
    AGAIN();
    acc[1] = *(const LDSP f4*)(P + lane * 4); acc[2] = *(const LDSP f4*)(P + 256 + lane * 4);
    const unsigned tag = tag_of(ep);
    int gqs = WS_GQ + h * GQ_STRIDE;
    PINS(gqs);
    const int rec = s + (s > 2) + (s > 5);
    const int to = (lane & 8) ? OFF : (lane >> 4) * 128 + c7 * 16;
    float q0, q1;
    if (s == 2 || s == 5) {
      fold_tile(s == 2 ? acc[2] : acc[1] + acc[2], q0, q1);
      __builtin_amdgcn_raw_buffer_store_b128(v4u{asu(q0), tag, asu(q1), tag}, rws, to, gqs + (rec + 1) * 512, AUX_SC1);
      fold_tile(s == 2 ? acc[0] + acc[1] : acc[0], q0, q1);
    } else fold_tile((acc[0] + acc[1]) + acc[2], q0, q1);
    __builtin_amdgcn_raw_buffer_store_b128(v4u{asu(q0), tag, asu(q1), tag}, rws, to, gqs + rec * 512, AUX_SC1);
    SB();
    __builtin_amdgcn_s_sleep(QDLY);
    POLL {
#pragma unroll
      for (int i = 0; i < 5; ++i) g[i] = __builtin_amdgcn_raw_buffer_load_b128(rws, lane * 16, gqs + i * 1024, AUX_SC1);
      if (none(miss(g[0], tag) | miss(g[1], tag) | miss(g[2], tag) | miss(g[3], tag) | miss(g[4], tag))) break;
      AGAIN();
    }
    {
      const bool hi = lane >= 32;
      float qa[3][2];
#pragma unroll
      for (int e = 0; e < 2; ++e) {
        qa[0][e] = asf(g[0][2 * e]) + (hi ? 0.f : asf(g[1][2 * e]));
        qa[1][e] = asf(g[2][2 * e]) + (hi ? asf(g[1][2 * e]) : asf(g[3][2 * e]));
        qa[2][e] = asf(g[4][2 * e]) + (hi ? asf(g[3][2 * e]) : 0.f);
      }
#pragma unroll
      for (int t = 0; t < 3; ++t) {
        float a0 = qa[t][0], a1 = qa[t][1], b0 = a0, b1 = a1;
        swap32(a0, b0); swap32(a1, b1);
        qs[32 * t + 4 * c7 + 2 * (r8 & 1) + ((r8 >> 1) & 1)] = pk_bf16(a0 + b0, a1 + b1);
      }
    }
    if (lane == 0) ctl[0] = tag;
    BARRIER();
    BARRIER();
    const unsigned sides = __builtin_amdgcn_readfirstlane(ctl[3]);
    int g1o = (r8 >> 2) * 16384 + (r8 & 3) * G1_STRIDE + c7 * 16, g1q = WS_G1 + (h & 15) * 32768 + SIDE(b & 7) + 3 * (s & 3) * 128;
    int mls = WS_ML + h * 1024;
    PINS(mls); PINS(g1q); PINV(g1o);
    const float mw = ms[c7], M = max8(mw);
    const float L = sum8(ls[c7] * __builtin_amdgcn_exp2f(mw - M));
    __builtin_amdgcn_raw_buffer_store_b128(v4u{asu(M), tag, asu(L), tag}, rws, lane < 8 ? lane * 16 : OFF, mls + s * 128, AUX_SC1);
    SB();
#pragma unroll
    for (int i = 0; i < 3; ++i) g[i] = __builtin_amdgcn_raw_buffer_load_b128(rws, g1o + i * 128, g1q, AUX_SC1);
    SB();
    __builtin_amdgcn_s_sleep(MDLY);
    asm volatile("" :: "v"(g[0]), "v"(g[1]), "v"(g[2]));
    POLL {
      g[3] = __builtin_amdgcn_raw_buffer_load_b128(rws, lane * 16, mls, AUX_SC1);
      if (none(miss(g[3], tag))) break;
      AGAIN();
    }
    const float Ms = asf(g[3][0]), Mg = max_hi(Ms);
    const float Lg = sum_hi(__builtin_amdgcn_exp2f(Ms - Mg) * asf(g[3][2]));
    if (lane == 0) ctl[1] = asu(__builtin_amdgcn_exp2f(M - Mg) / Lg);
    LDSP unsigned* ow0 = os + 8 * c7 + (lane >> 4); LDSP unsigned* ow1 = os + 8 * c7 + 4 + (lane >> 5);
    PINV(ow0); PINV(ow1);
    BARRIER();
    BARRIER();
    BARRIER();
    __builtin_amdgcn_s_sleep(H1DLY);
    POLL {
#pragma unroll
      for (int i = 0; i < 3; ++i) g[i] = __builtin_amdgcn_raw_buffer_load_b128(rws, g1o + i * 128, g1q, AUX_SC1);
      if (none(miss(g[0], tag) | miss(g[1], tag) | miss(g[2], tag))) break;
      AGAIN();
    }
    float v0 = asf(g[0][0]), v1 = asf(g[0][2]), v2 = asf(g[1][0]), v3 = asf(g[1][2]), v4 = asf(g[2][0]), v5 = asf(g[2][2]);
    swap32(v0, v2); v0 += v2;
    swap32(v1, v3); v1 += v3;
    swap32(v4, v5); v4 += v5;
    swap16(v0, v1); v0 += v1;
    v5 = v4; swap16(v4, v5); v4 += v5;
    v0 += dpp<ROR8>(v0); v4 += dpp<ROR8>(v4);
    *ow0 = pk_bf16(v0, v0); *ow1 = pk_bf16(v4, v4);
    BARRIER();
    __builtin_amdgcn_endpgm();
  }

  {
    const int w = wave - 8;
    unsigned kvo = (unsigned)(h * SK + (SK - W) + (w * 8 + ((s + w) & 7)) * 8) * (DH * 2);
    int lo = r8 * (DH * 2) + c7 * 16;
    PINV(lo); PINS(kvo);
    ARG3(kc, 1, vc, 2, ws0, 6);
    u64 ta; SB(); asm volatile("s_memrealtime %0\n\ts_waitcnt lgkmcnt(0)" : "=s"(ta) :: "memory"); SB();
    const rsrc_t rk = mk_rsrc((const char*)kc + kvo, (7 * 512 + 8) * DH * 2), rv = mk_rsrc((const char*)vc + kvo, (7 * 512 + 8) * DH * 2);
    v4u kv[48];
#define ISSUE(n) kv[n] = __builtin_amdgcn_raw_buffer_load_b128((n) < 24 ? rk : rv, lo + ((n) % 3) * 128, (((n) % 24) / 3) * 512 * DH * 2, AUX_NT)
#define STEP(n) do { SB(); asm volatile("s_waitcnt vmcnt(%0)" :: "n"(WIN - 1) : "memory"); ISSUE(n); SB(); } while (0)
#pragma unroll
    for (int n = 0; n < WIN0; ++n) ISSUE(n);
    BARRIER();
#pragma unroll
    for (int n = WIN0; n <= N_QB; ++n) STEP(n);
    BARRIER();
    v4u qv[3];
#pragma unroll
    for (int t = 0; t < 3; ++t) qv[t] = *(const LDSP v4u*)(qs + 32 * t + 4 * c7);
    const unsigned tag = ctl[0];
    constexpr float C2 = 0.07216878364870322f * 1.4426950408889634f;
    constexpr int N_SC = (N_QB + 7 > 23 + WIN) ? N_QB + 7 : 23 + WIN;
    float sc[NG];
#pragma unroll
    for (int n = N_QB; n <= N_SC; ++n) {
      if (n > N_QB) STEP(n);
#pragma unroll
      for (int j = 0; j < NG; ++j) {
        if (n == ((N_QB + j > 3 * j + 2 + WIN) ? N_QB + j : 3 * j + 2 + WIN)) {
          float a = 0.f;
#pragma unroll
          for (int t = 0; t < 3; ++t)
#pragma unroll
            for (int k = 0; k < 4; ++k) a = dot2(kv[3 * j + t][k], qv[t][k], a);
          sc[j] = sum8(a) * C2;
          PINV(sc[j]);
        }
      }
    }
    float m = sc[0];
#pragma unroll
    for (int j = 1; j < NG; ++j) m = fmaxf(m, sc[j]);
    m = max_hi(m);
    STEP(N_SC + 1);
    float l = 0.f;
#pragma unroll
    for (int j = 0; j < NG; ++j) { sc[j] = __builtin_amdgcn_exp2f(sc[j] - m); l += sc[j]; }
    l = sum_hi(l);
    if (lane == 0) { ms[w] = m; ls[w] = l; }
    STEP(N_SC + 2);
    BARRIER();
    const float wt = __builtin_amdgcn_exp2f(m - max8(ms[c7]));
    const v4u msk = pos_mask(c7);
    f4 acc[3] = {{0.f, 0.f, 0.f, 0.f}, {0.f, 0.f, 0.f, 0.f}, {0.f, 0.f, 0.f, 0.f}};
    v4u pa;
#define PV(n) do { if ((n) % 3 == 0) { const float p = sc[((n) - 24) / 3] * wt; pa = msk & pk_bf16(p, p); } acc[(n) % 3] = mfma(pa, kv[n], acc[(n) % 3]); } while (0)
#pragma unroll
    for (int n = 24; n <= N_SC + 2 - WIN; ++n) PV(n);
#pragma unroll
    for (int n = N_SC + 3; n < 48; ++n) { STEP(n); if (n == 47) BARRIER(); PV(n - WIN); }
    LDSP float* pp = P + (2 * w + (lane >> 5)) * PSTR + 8 * c7 + 4 * ((lane >> 4) & 1);
    const bool pvalid = (lane >> 5) == ((lane >> 3) & 1);
    const LDSP float* rp = P + 2 * r8 * PSTR + 32 * w + 4 * c7;
    const rsrc_t rws = mk_rsrc((void*)up2m(ws0), WS_END);
    const unsigned sides = __builtin_amdgcn_readfirstlane(ctl[3]);
    int so = (lane & 8) ? OFF : (lane >> 4) * 64 + c7 * 8;
    int ss = WS_G1 + (h & 15) * 32768 + SIDE(b & 7) + (s >> 2) * 16384 + (s & 3) * G1_STRIDE + w * 256;
    float f = asf(ctl[1]);
    v2u sv; sv[1] = tag;
    PINV(so); PINS(ss); PINV(f); PINV(pp); PINV(rp); PINV(sv);
#pragma unroll
    for (int n = 48 - WIN; n < 45; ++n) PV(n);
#pragma unroll
    for (int n = 45; n < 48; ++n) { if (n >= 48 - WIN) PV(n); if (pvalid) *(LDSP f4*)(pp + 64 * (n - 45)) = acc[n - 45]; SB(); }
    BARRIER();
    if (w < 6) {
      const f4 t = *(const LDSP f4*)rp + *(const LDSP f4*)(rp + PSTR);
      float t0 = t[0], t1 = t[1], t2 = t[2], t3 = t[3];
      swap32(t0, t2); t0 += t2;
      swap32(t1, t3); t1 += t3;
      swap16(t0, t1); t0 += t1;
      t0 += dpp<ROR8>(t0);
      sv[0] = asu(t0 * f);
      __builtin_amdgcn_raw_buffer_store_b64(sv, rws, so, ss, AUX_SC1);
    }
    BARRIER();
    BARRIER();
    if (w == 3 && (jb == 5)) { const u64 te = t00 + 4000 + (20 * (ta - tb)); u64 tn; do { __builtin_amdgcn_s_sleep(1); asm volatile("s_memrealtime %0\n\ts_waitcnt lgkmcnt(0)" : "=s"(tn) :: "memory"); } while (tn < te && tn < t00 + 50000); }
    __builtin_amdgcn_endpgm();
  }
}

extern "C" void kernel_launch(void* const* d_in, const int* in_sizes, int n_in, void* d_out, int out_size, void* d_ws, size_t ws_size, hipStream_t stream) {
  fused_decode<<<256, NW * 64, 0, stream>>>((const u16*)d_in[0], (const u16*)d_in[1], (const u16*)d_in[2], (const u16*)d_in[3], (const u16*)d_in[4],
                                            (u16*)d_out, (unsigned char*)d_ws, d_ws);
}
```

```cpp
#include <hip/hip_runtime.h>
#include <hip/hip_bf16.h>
#include <stdint.h>

typedef unsigned short u16;
typedef unsigned long long u64;
typedef unsigned v4u __attribute__((ext_vector_type(4)));
typedef unsigned v2u __attribute__((ext_vector_type(2)));
typedef float f4 __attribute__((ext_vector_type(4)));
typedef __bf16 bf16x2 __attribute__((ext_vector_type(2)));
typedef __bf16 bf16x8 __attribute__((ext_vector_type(8)));
typedef __amdgpu_buffer_rsrc_t rsrc_t;
#define LDSP __attribute__((address_space(3)))

constexpr int D = 640, H = 32, SK = 32768, DH = 192, W = 4096;
constexpr int NW = 16, NG = 8;
constexpr int WIN = 3;
constexpr int WIDE = 5;
constexpr int T0 = 110, DT = 292, N_AD = 8;
constexpr int N_QB = 20;
constexpr int QDLY = 62, MDLY = 70, EDLY = 105, H1DLY = 8, H2DLY = 13;
constexpr int CODE_BYTES = 9440;
constexpr unsigned SPIN_CAP = 1u << 20;
constexpr int AUX_NT = 2, AUX_SC1 = 16;
constexpr int OFF = 0x7ffffff0;

constexpr int WS_EPOCH = 0;
constexpr int WS_SD = 1024;
constexpr int WS_GQ = 4096;
constexpr int GQ_STRIDE = 10 * 512;
constexpr int WS_ML = WS_GQ + 32 * GQ_STRIDE;
constexpr int WS_PR = 224 << 10;
constexpr int WS_G1 = 256 << 10;
constexpr int G1_STRIDE = 13 * 128;
constexpr int WS_G2 = 768 << 10;
constexpr int WS_END = WS_G2 + 20 * 32768;
constexpr int PSTR = 208;

__device__ __forceinline__ rsrc_t mk_rsrc(const void* p, int bytes) { return __builtin_amdgcn_make_buffer_rsrc((void*)p, 0, bytes, 0x00020000); }
__device__ __forceinline__ u64 up2m(u64 p) { return (p + 0x1fffff) & ~0x1fffffull; }
__device__ __forceinline__ float asf(unsigned u) { return __builtin_bit_cast(float, u); }
__device__ __forceinline__ unsigned asu(float f) { return __builtin_bit_cast(unsigned, f); }
template <int CTRL> __device__ __forceinline__ float dpp(float x) { return asf(__builtin_amdgcn_mov_dpp(asu(x), CTRL, 0xf, 0xf, true)); }
constexpr int XOR1 = 0xB1, XOR2 = 0x4E, HMIR = 0x141, ROR8 = 0x128;
__device__ __forceinline__ unsigned pk_bf16(float lo, float hi) { bf16x2 r = {(__bf16)lo, (__bf16)hi}; return __builtin_bit_cast(unsigned, r); }
__device__ __forceinline__ float dot2(unsigned a, unsigned b, float c) { return __builtin_amdgcn_fdot2_f32_bf16(__builtin_bit_cast(bf16x2, a), __builtin_bit_cast(bf16x2, b), c, false); }
__device__ __forceinline__ f4 mfma(v4u a, v4u b, f4 c) { return __builtin_amdgcn_mfma_f32_16x16x32_bf16(__builtin_bit_cast(bf16x8, a), __builtin_bit_cast(bf16x8, b), c, 0, 0, 0); }
__device__ __forceinline__ float sum8(float x) { x += dpp<XOR1>(x); x += dpp<XOR2>(x); x += dpp<HMIR>(x); return x; }
__device__ __forceinline__ float max8(float x) { x = fmaxf(x, dpp<XOR1>(x)); x = fmaxf(x, dpp<XOR2>(x)); x = fmaxf(x, dpp<HMIR>(x)); return x; }
__device__ __forceinline__ void swap32(float& a, float& b) { auto r = __builtin_amdgcn_permlane32_swap(asu(a), asu(b), false, false); a = asf(r[0]); b = asf(r[1]); }
__device__ __forceinline__ void swap16(float& a, float& b) { auto r = __builtin_amdgcn_permlane16_swap(asu(a), asu(b), false, false); a = asf(r[0]); b = asf(r[1]); }
__device__ __forceinline__ float sum_hi(float x) { x += dpp<ROR8>(x); float y = x; swap16(x, y); x += y; y = x; swap32(x, y); return x + y; }
__device__ __forceinline__ float max_hi(float x) { x = fmaxf(x, dpp<ROR8>(x)); float y = x; swap16(x, y); x = fmaxf(x, y); y = x; swap32(x, y); return fmaxf(x, y); }
__device__ __forceinline__ unsigned miss(v4u g, unsigned tag) { return (g[1] ^ tag) | (g[3] ^ tag); }
__device__ __forceinline__ bool none(unsigned x) { return __builtin_amdgcn_ballot_w64(x != 0) == 0; }
__device__ __forceinline__ unsigned tag_of(unsigned epoch) { return __builtin_amdgcn_readfirstlane(epoch) * 0x9E3779B1u + 0x7F4A7C15u; }
__device__ __forceinline__ v4u pos_mask(int c7) {
  v4u m;
#pragma unroll
  for (int d = 0; d < 4; ++d) m[d] = ((c7 >> 1) == d) ? ((c7 & 1) ? 0xffff0000u : 0x0000ffffu) : 0u;
  return m;
}
__device__ __forceinline__ void fold_tile(f4 d, float& o0, float& o1) {
  float a = d[0], b = d[2], c = d[1], e = d[3];
  swap32(a, b); swap32(c, e);
  o0 = a + dpp<ROR8>(b); o1 = c + dpp<ROR8>(e);
}

#define SB() __builtin_amdgcn_sched_barrier(0)
#define BARRIER() do { SB(); asm volatile("s_waitcnt lgkmcnt(0)\n\ts_barrier" ::: "memory"); SB(); } while (0)
#define PINV(x) asm volatile("" : "+v"(x))
#define PINS(x) asm volatile("" : "+s"(x))
#define POLL _Pragma("nounroll") for (unsigned spin = 0; spin < SPIN_CAP; ++spin)
#define AGAIN() asm volatile("" ::: "memory")
#define V64_77 "v64", "v65", "v66", "v67", "v68", "v69", "v70", "v71", "v72", "v73", "v74", "v75", "v76", "v77"
#define V78_83 "v78", "v79", "v80", "v81", "v82", "v83"
#define V84_103 "v84", "v85", "v86", "v87", "v88", "v89", "v90", "v91", "v92", "v93", "v94", "v95", "v96", "v97", "v98", "v99", "v100", "v101", "v102", "v103"

__global__ void __launch_bounds__(NW * 64, 4) fused_decode(const u16*, const u16*, const u16*, const u16*, const u16*, u16*, unsigned char*, const void*) {
  __shared__ __attribute__((aligned(16))) float lds[16 * PSTR + 96 + 64 + 8 + 8 + 4];
  LDSP float* P = (LDSP float*)lds;
  LDSP unsigned* qs = (LDSP unsigned*)(P + 16 * PSTR);
  LDSP unsigned* os = qs + 96;
  LDSP float* ms = P + 16 * PSTR + 96 + 64;
  LDSP float* ls = ms + 8;
  LDSP unsigned* ctl = (LDSP unsigned*)(ls + 8);
  asm volatile("s_load_dwordx16 s[64:79], %0, 0x0" :: "s"(__builtin_amdgcn_kernarg_segment_ptr())
               : "s64", "s65", "s66", "s67", "s68", "s69", "s70", "s71", "s72", "s73", "s74", "s75", "s76", "s77", "s78", "s79");
#define ARG3(p0, i0, p1, i1, p2, i2) u64 p0, p1, p2; \
  asm volatile("s_waitcnt lgkmcnt(0)\n\ts_mov_b64 %0, s[%c3:%c4]\n\ts_mov_b64 %1, s[%c5:%c6]\n\ts_mov_b64 %2, s[%c7:%c8]" : "=s"(p0), "=s"(p1), "=s"(p2) \
               : "i"(64 + 2 * i0), "i"(65 + 2 * i0), "i"(64 + 2 * i1), "i"(65 + 2 * i1), "i"(64 + 2 * i2), "i"(65 + 2 * i2))
  unsigned pf = 0;
  if (threadIdx.x < 128) {
    u64 pc; asm volatile("s_getpc_b64 %0" : "=s"(pc));
    pf = __builtin_amdgcn_raw_buffer_load_b32(mk_rsrc((void*)(pc & ~127ull), CODE_BYTES), threadIdx.x * 128 + 128, 0, 0);
  }
  SB();
  const int b = blockIdx.x, jb = b >> 3;
  const int h = (b & 7) * 4 + (jb >> 3), s = jb & 7;
  const int tid = threadIdx.x, wave = __builtin_amdgcn_readfirstlane(tid >> 6), lane = tid & 63, r8 = lane >> 3, c7 = lane & 7;
#define SIDE(c) (((sides >> (c)) & 1) << 13)

  if (wave >= 3 && wave < 8) {
    const int rq = s & 3, ch = s >> 2;
    ARG3(wo, 4, out, 5, ws0, 6);
    const u64 ws = up2m(ws0);
    const rsrc_t rws = mk_rsrc((void*)ws, WS_END), rout = mk_rsrc((void*)out, D * 2);
    unsigned ep = 0;
    if (b < 8) {
      __builtin_amdgcn_raw_buffer_store_b128(v4u{0u, 0u, 0u, 0u}, rws, lane < 16 ? r8 * 8192 + c7 * 16 : OFF, WS_PR + b * 1024 + wave * 128, AUX_SC1);
      ep = __builtin_amdgcn_raw_buffer_load_b32(rws, 0, WS_EPOCH, AUX_SC1);
    }
    BARRIER();
    const rsrc_t ro = mk_rsrc((const u16*)wo + (size_t)(h * DH) * D + 320 * ch + 64 * (wave - 3), DH * D * 2);
    v4u msk = pos_mask(c7);
    int wl = (8 * (r8 & 3) + (r8 >> 2)) * (D * 2) + c7 * 16;
    int wsf[3];
#pragma unroll
    for (int i = 0; i < 3; ++i) { const int n = 3 * rq + i; wsf[i] = (32 * (n >> 1) + 2 * (n & 1)) * (D * 2); PINS(wsf[i]); }
    const int yl = 4 * (wave - 3) + (lane >> 4), yp = h * 4 + rq;
    int ys = WS_G2 + (yp >> 6) * 16384 + (yp & 63) * 128;
    const int lc = (b & 7) * 5 + (jb >> 2), qd = jb & 3;
    const bool fin = wave == 4 && jb < 20;
    int fo = (lane >> 1) * 128 + (lane & 1) * 16;
    int oo = (lane & 30) ? OFF : 2 * (64 * (lc >> 2) + 8 * (2 * qd + (lane & 1)) + 4 * (lc & 1) + (lc & 2) + (lane >> 5));
    const LDSP unsigned* op = os + 8 * r8;
    PINV(fo); PINV(oo); PINV(wl); PINV(msk); PINV(op); PINS(ys);
    if (b < 8) {
      asm volatile("s_waitcnt vmcnt(0)" ::: "memory");
      const int first = (wave ^ b) & 1, o0 = WS_PR + b * 1024 + wave * 128 + first * 8192, o1 = o0 ^ 8192;
      u64 c0, c1, c2; unsigned unused;
      asm volatile("s_memtime %1\n\ts_waitcnt lgkmcnt(0)\n\ts_load_dword %0, %4, %5\n\ts_waitcnt lgkmcnt(0)\n\ts_memtime %2\n\ts_waitcnt lgkmcnt(0)\n\t"
                   "s_load_dword %0, %4, %6\n\ts_waitcnt lgkmcnt(0)\n\ts_memtime %3\n\ts_waitcnt lgkmcnt(0)"
                   : "=&s"(unused), "=&s"(c0), "=&s"(c1), "=&s"(c2) : "s"(ws), "s"(o0), "s"(o1) : "memory");
      const int hi = (__builtin_amdgcn_s_getreg(20 | (3 << 11)) >> 2) & 1;
      int d = max(-1000, min(1000, (int)(c1 - c0) - (int)(c2 - c1)));
      if (first ^ hi) d = -d;
      __builtin_amdgcn_raw_buffer_store_b64(v2u{(unsigned)(2 * d + hi), tag_of(ep)}, rws, lane ? OFF : b * 128 + wave * 8, WS_SD, AUX_SC1);
    }
    BARRIER();
    const unsigned tag = __builtin_amdgcn_readfirstlane(ctl[0]);
    if (wave == 7) {
      v2u sd;
      POLL {
        sd = __builtin_amdgcn_raw_buffer_load_b64(rws, r8 * 128 + max(c7, 3) * 8, WS_SD, AUX_SC1);
        if (none(sd[1] ^ tag)) break;
        AGAIN();
      }
      const float dd = sum_hi(sum8(c7 >= 3 ? (float)((int)sd[0] >> 1) : 0.f));
      const u64 bl = __builtin_amdgcn_ballot_w64(((sd[0] & 1) != 0) != (dd > 0.f));
      const unsigned sb = (bl & 1) | ((bl >> 7) & 2) | ((bl >> 14) & 4) | ((bl >> 21) & 8);
      if (lane == 0) ctl[3] = sb | ((sb ^ 15u) << 4);
    }
    BARRIER();
    const unsigned sides = __builtin_amdgcn_readfirstlane(ctl[3]);
    int fs[4];
#pragma unroll
    for (int i = 0; i < 4; ++i) { fs[i] = WS_G2 + (lc % 20) * 32768 + (i >> 1) * 16384 + SIDE(b & 7) + (i & 1) * 4096 + qd * 32; PINS(fs[i]); }
    int yo = (lane & 8) ? OFF : yl * 32768 + SIDE(4 * ch + yl / 5) + c7 * 16;
    PINV(yo);
    if (fin) {
      v4u g[4];
#pragma unroll
      for (int i = 0; i < 4; ++i) g[i] = __builtin_amdgcn_raw_buffer_load_b128(rws, fo, fs[i], AUX_SC1);
      asm volatile("" :: "v"(g[0]), "v"(g[1]), "v"(g[2]), "v"(g[3]));
    }
    BARRIER();
    v4u wr[6];
#pragma unroll
    for (int j = 0; j < 6; ++j) wr[j] = __builtin_amdgcn_raw_buffer_load_b128(ro, wl, wsf[j >> 1] + (j & 1) * 4 * (D * 2), AUX_NT);
    BARRIER();
    BARRIER();
    asm volatile(
        "v_mov_b32 v101, %[tg]\n\tv_mov_b32 v103, %[tg]\n\t"
        "s_barrier\n\t"
        "ds_read_b128 v[64:67], %[op]\n\tds_read_b64 v[68:69], %[op] offset:16\n\t"
        "s_waitcnt lgkmcnt(1)\n\t"
        "v_and_b32 v72, v64, %[m0]\n\tv_and_b32 v73, v64, %[m1]\n\tv_and_b32 v74, v64, %[m2]\n\tv_and_b32 v75, v64, %[m3]\n\t"
        "v_and_b32 v76, v65, %[m0]\n\tv_and_b32 v77, v65, %[m1]\n\t"
        "v_mfma_f32_16x16x32_bf16 v[96:99], v[72:75], %[w0], 0\n\t"
        "v_and_b32 v78, v65, %[m2]\n\tv_and_b32 v79, v65, %[m3]\n\tv_and_b32 v80, v66, %[m0]\n\tv_and_b32 v81, v66, %[m1]\n\t"
        "v_mfma_f32_16x16x32_bf16 v[96:99], v[76:79], %[w1], v[96:99]\n\t"
        "v_and_b32 v82, v66, %[m2]\n\tv_and_b32 v83, v66, %[m3]\n\tv_and_b32 v84, v67, %[m0]\n\tv_and_b32 v85, v67, %[m1]\n\t"
        "v_mfma_f32_16x16x32_bf16 v[96:99], v[80:83], %[w2], v[96:99]\n\t"
        "v_and_b32 v86, v67, %[m2]\n\tv_and_b32 v87, v67, %[m3]\n\ts_waitcnt lgkmcnt(0)\n\tv_and_b32 v88, v68, %[m0]\n\tv_and_b32 v89, v68, %[m1]\n\t"
        "v_mfma_f32_16x16x32_bf16 v[96:99], v[84:87], %[w3], v[96:99]\n\t"
        "v_and_b32 v90, v68, %[m2]\n\tv_and_b32 v91, v68, %[m3]\n\tv_and_b32 v92, v69, %[m0]\n\tv_and_b32 v93, v69, %[m1]\n\t"
        "v_mfma_f32_16x16x32_bf16 v[96:99], v[88:91], %[w4], v[96:99]\n\t"
        "v_and_b32 v94, v69, %[m2]\n\tv_and_b32 v95, v69, %[m3]\n\ts_nop 1\n\t"
        "v_mfma_f32_16x16x32_bf16 v[96:99], v[92:95], %[w5], v[96:99]\n\t"
        "s_nop 7\n\t"
        "v_permlane32_swap_b32 v96, v98\n\tv_permlane32_swap_b32 v97, v99\n\ts_nop 0\n\t"
        "v_add_f32_dpp v100, v98, v96 row_ror:8 row_mask:0xf bank_mask:0xf\n\tv_add_f32_dpp v102, v99, v97 row_ror:8 row_mask:0xf bank_mask:0xf\n\t"
        "buffer_store_dwordx4 v[100:103], %[yo], %[rs], %[ys] offen sc1\n\ts_nop 1"
        :: [tg] "s"(tag), [op] "v"(op), [m0] "v"(msk[0]), [m1] "v"(msk[1]), [m2] "v"(msk[2]), [m3] "v"(msk[3]),
           [w0] "v"(wr[0]), [w1] "v"(wr[1]), [w2] "v"(wr[2]), [w3] "v"(wr[3]), [w4] "v"(wr[4]), [w5] "v"(wr[5]), [yo] "v"(yo), [rs] "s"(rws), [ys] "s"(ys)
        : "memory", V64_77, V78_83, V84_103);
    if (fin) {
      __builtin_amdgcn_s_sleep(H2DLY);
      unsigned cnt = SPIN_CAP;
      asm volatile(
          "1:\n\ts_nop 2\n\t"
          "buffer_load_dwordx4 v[64:67], %[fo], %[rs], %[f0] offen sc1\n\tbuffer_load_dwordx4 v[68:71], %[fo], %[rs], %[f1] offen sc1\n\t"
          "buffer_load_dwordx4 v[72:75], %[fo], %[rs], %[f2] offen sc1\n\tbuffer_load_dwordx4 v[76:79], %[fo], %[rs], %[f3] offen sc1\n\t"
          "s_sub_u32 %[cnt], %[cnt], 1\n\t"
          "s_waitcnt vmcnt(3)\n\tv_bitop3_b32 v65, v65, v67, %[tg] bitop3:0x7e\n\t"
          "s_waitcnt vmcnt(2)\n\tv_bitop3_b32 v69, v69, v71, %[tg] bitop3:0x7e\n\tv_add_f32 v80, v64, v68\n\tv_add_f32 v81, v66, v70\n\t"
          "s_waitcnt vmcnt(1)\n\tv_bitop3_b32 v73, v73, v75, %[tg] bitop3:0x7e\n\tv_or3_b32 v65, v65, v69, v73\n\t"
          "s_waitcnt vmcnt(0)\n\tv_bitop3_b32 v77, v77, v79, %[tg] bitop3:0x7e\n\tv_add_f32 v72, v72, v76\n\tv_add_f32 v74, v74, v78\n\t"
          "v_or_b32 v65, v65, v77\n\tv_add_f32 v80, v80, v72\n\tv_add_f32 v81, v81, v74\n\t"
          "v_cmpx_ne_u32 vcc, 0, v65\n\t"
          "s_cbranch_scc1 2f\n\ts_cbranch_execnz 1b\n"
          "2:\n\ts_mov_b64 exec, -1\n\ts_nop 1\n\t"
          "v_permlane32_swap_b32 v80, v81\n\tv_add_f32 v82, v80, v81\n\tv_add_f32 v83, v80, v81\n\ts_nop 1\n\t"
          "v_permlane16_swap_b32 v82, v83\n\tv_add_f32 v80, v82, v83\n\ts_nop 1\n\t"
          "v_add_f32_dpp v80, v80, v80 row_ror:8 row_mask:0xf bank_mask:0xf\n\ts_nop 1\n\t"
          "v_add_f32_dpp v80, v80, v80 row_ror:4 row_mask:0xf bank_mask:0xf\n\ts_nop 1\n\t"
          "v_add_f32_dpp v80, v80, v80 quad_perm:[2,3,0,1] row_mask:0xf bank_mask:0xf\n\t"
          "v_cvt_pk_bf16_f32 v80, v80, v80\n\t"
          "buffer_store_short v80, %[oo], %[ro], 0 offen"
          : [cnt] "+s"(cnt)
          : [fo] "v"(fo), [rs] "s"(rws), [f0] "s"(fs[0]), [f1] "s"(fs[1]), [f2] "s"(fs[2]), [f3] "s"(fs[3]), [tg] "s"(tag), [oo] "v"(oo), [ro] "s"(rout)
          : "memory", "vcc", "scc", V64_77, V78_83);
    }
    __builtin_amdgcn_endpgm();
  }

  if (wave < 3) {
    const int u = 3 * s + wave;
    int wl = r8 * 10 * (H * DH * 2) + c7 * 16, xl = 20 * r8 + (u & 7) * 160;
    unsigned us = (u & 7) * 80 * (H * DH * 2) + (u >> 3) * 128 + h * (DH * 2);
    PINV(wl); PINV(xl); PINS(us);
    ARG3(wq, 3, x, 0, ws0, 6);
    const rsrc_t rq = mk_rsrc((const char*)wq + us, 79 * H * DH * 2 + 128), rx = mk_rsrc((void*)x, D * 2), rws = mk_rsrc((void*)up2m(ws0), WS_END);
    v4u wr[10], g[5];
#pragma unroll
    for (int j = 0; j < 10; ++j) wr[j] = __builtin_amdgcn_raw_buffer_load_b128(rq, wl, j * (H * DH * 2), AUX_NT);
    const v4u xa = __builtin_amdgcn_raw_buffer_load_b128(rx, xl, 0, 0);
    const unsigned xb = __builtin_amdgcn_raw_buffer_load_b32(rx, xl + 16, 0, 0);
    const unsigned ep = __builtin_amdgcn_raw_buffer_load_b32(rws, wave ? OFF : 0, WS_EPOCH, AUX_SC1);
    if (tid == 0) ctl[2] = 0;
    BARRIER();
    const v4u msk = pos_mask(c7);
    f4 acc[3];
    acc[0] = f4{0.f, 0.f, 0.f, 0.f};
#pragma unroll
    for (int j = 0; j < 10; ++j) {
      const unsigned xw = j < 8 ? xa[j >> 1] : xb;
      acc[0] = mfma(msk & __builtin_amdgcn_perm(xw, xw, (j & 1) ? 0x03020302u : 0x01000100u), wr[j], acc[0]);
    }
    asm volatile("" :: "v"(pf));
    if (wave) {
      *(LDSP f4*)(P + (wave - 1) * 256 + lane * 4) = acc[0];
      asm volatile("s_waitcnt lgkmcnt(0)" ::: "memory");
      if (lane == 0) __hip_atomic_fetch_add(ctl + 2, 1u, __ATOMIC_RELAXED, __HIP_MEMORY_SCOPE_WORKGROUP);
      if (wave == 2 || b != 0) __builtin_amdgcn_endpgm();
      BARRIER();
      const unsigned tag = __builtin_amdgcn_readfirstlane(ctl[0]);
      BARRIER();
      __builtin_amdgcn_s_sleep(EDLY);
      POLL {
        if (none(miss(__builtin_amdgcn_raw_buffer_load_b128(rws, (lane & 31) * 1024, WS_ML, AUX_SC1), tag))) break;
        __builtin_amdgcn_s_sleep(8);
        AGAIN();
      }
      __builtin_amdgcn_raw_buffer_store_b32(tag, rws, lane ? OFF : 0, WS_EPOCH, AUX_SC1);
      BARRIER();
      __builtin_amdgcn_endpgm();
    }
    while (__hip_atomic_load(ctl + 2, __ATOMIC_RELAXED, __HIP_MEMORY_SCOPE_WORKGROUP) != 2u) __builtin_amdgcn_s_sleep(1);
    AGAIN();
    acc[1] = *(const LDSP f4*)(P + lane * 4); acc[2] = *(const LDSP f4*)(P + 256 + lane * 4);
    const unsigned tag = tag_of(ep);
    int gqs = WS_GQ + h * GQ_STRIDE;
    PINS(gqs);
    const int rec = s + (s > 2) + (s > 5);
    const int to = (lane & 8) ? OFF : (lane >> 4) * 128 + c7 * 16;
    float q0, q1;
    if (s == 2 || s == 5) {
      fold_tile(s == 2 ? acc[2] : acc[1] + acc[2], q0, q1);
      __builtin_amdgcn_raw_buffer_store_b128(v4u{asu(q0), tag, asu(q1), tag}, rws, to, gqs + (rec + 1) * 512, AUX_SC1);
      fold_tile(s == 2 ? acc[0] + acc[1] : acc[0], q0, q1);
    } else fold_tile((acc[0] + acc[1]) + acc[2], q0, q1);
    __builtin_amdgcn_raw_buffer_store_b128(v4u{asu(q0), tag, asu(q1), tag}, rws, to, gqs + rec * 512, AUX_SC1);
    SB();
    __builtin_amdgcn_s_sleep(QDLY);
    POLL {
#pragma unroll
      for (int i = 0; i < 5; ++i) g[i] = __builtin_amdgcn_raw_buffer_load_b128(rws, lane * 16, gqs + i * 1024, AUX_SC1);
      if (none(miss(g[0], tag) | miss(g[1], tag) | miss(g[2], tag) | miss(g[3], tag) | miss(g[4], tag))) break;
      AGAIN();
    }
    {
      const bool hi = lane >= 32;
      float qa[3][2];
#pragma unroll
      for (int e = 0; e < 2; ++e) {
        qa[0][e] = asf(g[0][2 * e]) + (hi ? 0.f : asf(g[1][2 * e]));
        qa[1][e] = asf(g[2][2 * e]) + (hi ? asf(g[1][2 * e]) : asf(g[3][2 * e]));
        qa[2][e] = asf(g[4][2 * e]) + (hi ? asf(g[3][2 * e]) : 0.f);
      }
#pragma unroll
      for (int t = 0; t < 3; ++t) {
        float a0 = qa[t][0], a1 = qa[t][1], b0 = a0, b1 = a1;
        swap32(a0, b0); swap32(a1, b1);
        qs[32 * t + 4 * c7 + 2 * (r8 & 1) + ((r8 >> 1) & 1)] = pk_bf16(a0 + b0, a1 + b1);
      }
    }
    if (lane == 0) ctl[0] = tag;
    BARRIER();
    BARRIER();
    const unsigned sides = __builtin_amdgcn_readfirstlane(ctl[3]);
    int g1o = (r8 >> 2) * 16384 + (r8 & 3) * G1_STRIDE + c7 * 16, g1q = WS_G1 + (h & 15) * 32768 + SIDE(b & 7) + 3 * (s & 3) * 128;
    int mls = WS_ML + h * 1024;
    PINS(mls); PINS(g1q); PINV(g1o);
    const float mw = ms[c7], M = max8(mw);
    const float L = sum8(ls[c7] * __builtin_amdgcn_exp2f(mw - M));
    __builtin_amdgcn_raw_buffer_store_b128(v4u{asu(M), tag, asu(L), tag}, rws, lane < 8 ? lane * 16 : OFF, mls + s * 128, AUX_SC1);
    SB();
#pragma unroll
    for (int i = 0; i < 3; ++i) g[i] = __builtin_amdgcn_raw_buffer_load_b128(rws, g1o + i * 128, g1q, AUX_SC1);
    SB();
    __builtin_amdgcn_s_sleep(MDLY);
    asm volatile("" :: "v"(g[0]), "v"(g[1]), "v"(g[2]));
    POLL {
      g[3] = __builtin_amdgcn_raw_buffer_load_b128(rws, lane * 16, mls, AUX_SC1);
      if (none(miss(g[3], tag))) break;
      AGAIN();
    }
    const float Ms = asf(g[3][0]), Mg = max_hi(Ms);
    const float Lg = sum_hi(__builtin_amdgcn_exp2f(Ms - Mg) * asf(g[3][2]));
    if (lane == 0) ctl[1] = asu(__builtin_amdgcn_exp2f(M - Mg) / Lg);
    LDSP unsigned* ow0 = os + 8 * c7 + (lane >> 4); LDSP unsigned* ow1 = os + 8 * c7 + 4 + (lane >> 5);
    PINV(ow0); PINV(ow1);
    BARRIER();
    BARRIER();
    BARRIER();
    __builtin_amdgcn_s_sleep(H1DLY);
    unsigned cnt = SPIN_CAP;
    asm volatile(
        "1:\n\t"
        "buffer_load_dwordx4 v[64:67], %[go], %[rs], %[gq] offen sc1\n\tbuffer_load_dwordx4 v[68:71], %[go], %[rs], %[gq] offen offset:128 sc1\n\t"
        "buffer_load_dwordx4 v[72:75], %[go], %[rs], %[gq] offen offset:256 sc1\n\t"
        "s_sub_u32 %[cnt], %[cnt], 1\n\t"
        "s_waitcnt vmcnt(2)\n\tv_bitop3_b32 v65, v65, v67, %[tg] bitop3:0x7e\n\t"
        "s_waitcnt vmcnt(1)\n\tv_bitop3_b32 v69, v69, v71, %[tg] bitop3:0x7e\n\t"
        "v_permlane32_swap_b32 v64, v68\n\tv_permlane32_swap_b32 v66, v70\n\tv_add_f32 v64, v64, v68\n\tv_add_f32 v66, v66, v70\n\t"
        "s_waitcnt vmcnt(0)\n\tv_permlane32_swap_b32 v72, v74\n\tv_bitop3_b32 v73, v73, v75, %[tg] bitop3:0x7e\n\t"
        "v_add_f32 v76, v72, v74\n\tv_add_f32 v77, v72, v74\n\tv_or3_b32 v65, v65, v69, v73\n\t"
        "v_permlane16_swap_b32 v64, v66\n\tv_cmp_ne_u32 vcc, 0, v65\n\tv_permlane16_swap_b32 v76, v77\n\t"
        "v_add_f32 v64, v64, v66\n\tv_add_f32 v76, v76, v77\n\t"
        "s_cbranch_scc1 2f\n\ts_cbranch_vccnz 1b\n"
        "2:\n\ts_nop 0\n\t"
        "v_add_f32_dpp v64, v64, v64 row_ror:8 row_mask:0xf bank_mask:0xf\n\tv_add_f32_dpp v76, v76, v76 row_ror:8 row_mask:0xf bank_mask:0xf\n\t"
        "v_cvt_pk_bf16_f32 v64, v64, v64\n\tv_cvt_pk_bf16_f32 v76, v76, v76\n\t"
        "ds_write_b32 %[o0], v64\n\tds_write_b32 %[o1], v76"
        : [cnt] "+s"(cnt)
        : [go] "v"(g1o), [rs] "s"(rws), [gq] "s"(g1q), [tg] "s"(tag), [o0] "v"(ow0), [o1] "v"(ow1)
        : "memory", "vcc", "scc", V64_77);
    BARRIER();
    __builtin_amdgcn_endpgm();
  }

  {
    const int w = wave - 8;
    unsigned kvo = (unsigned)(h * SK + (SK - W) + (w * 8 + ((s + w) & 7)) * 8) * (DH * 2);
    int lo = r8 * (DH * 2) + c7 * 16;
    PINV(lo); PINS(kvo);
    ARG3(kc, 1, vc, 2, ws0, 6);
    const rsrc_t rk = mk_rsrc((const char*)kc + kvo, (7 * 512 + 8) * DH * 2), rv = mk_rsrc((const char*)vc + kvo, (7 * 512 + 8) * DH * 2);
    v4u kv[48];
    const unsigned start = (unsigned)__builtin_amdgcn_s_memrealtime();
#define ISSUE(n) kv[n] = __builtin_amdgcn_raw_buffer_load_b128((n) < 24 ? rk : rv, lo + ((n) % 3) * 128, (((n) % 24) / 3) * 512 * DH * 2, AUX_NT)
    int late = 0;
#define STEP(n) do { SB(); \
      if ((n) <= N_AD) asm volatile("s_waitcnt vmcnt(%0)" :: "n"(WIN - 1) : "memory"); \
      else asm volatile("s_cmp_gt_i32 %0, 0\n\ts_waitcnt vmcnt(%1)\n\ts_cbranch_scc1 1f\n\ts_waitcnt vmcnt(%2)\n1:" :: "s"(late), "n"(WIDE - 1), "n"(WIN - 1) : "memory", "scc"); \
      ISSUE(n); SB(); \
      if ((n) >= N_AD && (n) % 3 == 2 && (n) < 47) { late = (int)((unsigned)__builtin_amdgcn_s_memrealtime() - start) - (T0 + (n) * DT / 10); PINS(late); SB(); } } while (0)
#pragma unroll
    for (int n = 0; n < WIN; ++n) ISSUE(n);
    BARRIER();
#pragma unroll
    for (int n = WIN; n <= N_QB; ++n) STEP(n);
    BARRIER();
    v4u qv[3];
#pragma unroll
    for (int t = 0; t < 3; ++t) qv[t] = *(const LDSP v4u*)(qs + 32 * t + 4 * c7);
    const unsigned tag = ctl[0];
    constexpr float C2 = 0.07216878364870322f * 1.4426950408889634f;
    constexpr int N_SC = (N_QB + 7 > 23 + WIDE) ? N_QB + 7 : 23 + WIDE;
    float sc[NG];
#pragma unroll
    for (int n = N_QB; n <= N_SC; ++n) {
      if (n > N_QB) STEP(n);
#pragma unroll
      for (int j = 0; j < NG; ++j) {
        if (n == ((N_QB + j > 3 * j + 2 + WIDE) ? N_QB + j : 3 * j + 2 + WIDE)) {
          float a = 0.f;
#pragma unroll
          for (int t = 0; t < 3; ++t)
#pragma unroll
            for (int k = 0; k < 4; ++k) a = dot2(kv[3 * j + t][k], qv[t][k], a);
          sc[j] = sum8(a) * C2;
          PINV(sc[j]);
        }
      }
    }
    float m = sc[0];
#pragma unroll
    for (int j = 1; j < NG; ++j) m = fmaxf(m, sc[j]);
    m = max_hi(m);
    STEP(N_SC + 1);
    float l = 0.f;
#pragma unroll
    for (int j = 0; j < NG; ++j) { sc[j] = __builtin_amdgcn_exp2f(sc[j] - m); l += sc[j]; }
    l = sum_hi(l);
    if (lane == 0) { ms[w] = m; ls[w] = l; }
    STEP(N_SC + 2);
    BARRIER();
    const float wt = __builtin_amdgcn_exp2f(m - max8(ms[c7]));
    const v4u msk = pos_mask(c7);
    f4 acc[3] = {{0.f, 0.f, 0.f, 0.f}, {0.f, 0.f, 0.f, 0.f}, {0.f, 0.f, 0.f, 0.f}};
    v4u pa;
#define PV(n) do { if ((n) % 3 == 0) { const float p = sc[((n) - 24) / 3] * wt; pa = msk & pk_bf16(p, p); } acc[(n) % 3] = mfma(pa, kv[n], acc[(n) % 3]); } while (0)
#pragma unroll
    for (int n = 24; n <= N_SC + 2 - WIDE; ++n) PV(n);
#pragma unroll
    for (int n = N_SC + 3; n < 48; ++n) { STEP(n); if (n == 47) BARRIER(); PV(n - WIDE); }
    LDSP float* pp = P + (2 * w + (lane >> 5)) * PSTR + 8 * c7 + 4 * ((lane >> 4) & 1);
    const bool pvalid = (lane >> 5) == ((lane >> 3) & 1);
    const LDSP float* rp = P + 2 * r8 * PSTR + 32 * w + 4 * c7;
    const rsrc_t rws = mk_rsrc((void*)up2m(ws0), WS_END);
    const unsigned sides = __builtin_amdgcn_readfirstlane(ctl[3]);
    int so = (lane & 8) ? OFF : (lane >> 4) * 64 + c7 * 8;
    int ss = WS_G1 + (h & 15) * 32768 + SIDE(b & 7) + (s >> 2) * 16384 + (s & 3) * G1_STRIDE + w * 256;
    float f = asf(ctl[1]);
    v2u sv; sv[1] = tag;
    PINV(so); PINS(ss); PINV(f); PINV(pp); PINV(rp); PINV(sv);
#pragma unroll
    for (int n = 48 - WIDE; n < 45; ++n) PV(n);
#pragma unroll
    for (int n = 45; n < 48; ++n) { PV(n); if (pvalid) *(LDSP f4*)(pp + 64 * (n - 45)) = acc[n - 45]; SB(); }
    BARRIER();
    if (w < 6) {
      const f4 t = *(const LDSP f4*)rp + *(const LDSP f4*)(rp + PSTR);
      float t0 = t[0], t1 = t[1], t2 = t[2], t3 = t[3];
      swap32(t0, t2); t0 += t2;
      swap32(t1, t3); t1 += t3;
      swap16(t0, t1); t0 += t1;
      t0 += dpp<ROR8>(t0);
      sv[0] = asu(t0 * f);
      __builtin_amdgcn_raw_buffer_store_b64(sv, rws, so, ss, AUX_SC1);
    }
    BARRIER();
    __builtin_amdgcn_endpgm();
  }
}

extern "C" void kernel_launch(void* const* d_in, const int* in_sizes, int n_in, void* d_out, int out_size, void* d_ws, size_t ws_size, hipStream_t stream) {
  fused_decode<<<256, NW * 64, 0, stream>>>((const u16*)d_in[0], (const u16*)d_in[1], (const u16*)d_in[2], (const u16*)d_in[3], (const u16*)d_in[4],
                                            (u16*)d_out, (unsigned char*)d_ws, d_ws);
}
```

```cpp
#include <hip/hip_runtime.h>
#include <hip/hip_bf16.h>
#include <stdint.h>

typedef unsigned short u16;
typedef unsigned long long u64;
typedef unsigned v4u __attribute__((ext_vector_type(4)));
typedef unsigned v2u __attribute__((ext_vector_type(2)));
typedef float f4 __attribute__((ext_vector_type(4)));
typedef __bf16 bf16x2 __attribute__((ext_vector_type(2)));
typedef __bf16 bf16x8 __attribute__((ext_vector_type(8)));
typedef __amdgpu_buffer_rsrc_t rsrc_t;
#define LDSP __attribute__((address_space(3)))

constexpr int D = 640, H = 32, SK = 32768, DH = 192, W = 4096;
constexpr int NW = 16, NG = 8;
constexpr int WIN = 3;
constexpr int WIDE = 5;
constexpr int T0 = 110, DT = 292, N_AD = 8;
constexpr int N_QB = 20;
constexpr int QDLY = 12, MDLY = 70, H1DLY = 8, H2DLY = 13;
constexpr unsigned QT = 500;
constexpr int CODE_BYTES = 9600;
constexpr unsigned SPIN_CAP = 1u << 20;
constexpr int AUX_NT = 2, AUX_SC1 = 16;
constexpr int OFF = 0x7ffffff0;

constexpr int WS_EPOCH = 0;
constexpr int WS_SD = 1024;
constexpr int WS_GQ = 4096;
constexpr int GQ_STRIDE = 10 * 512;
constexpr int WS_ML = WS_GQ + 32 * GQ_STRIDE;
constexpr int WS_PR = 224 << 10;
constexpr int WS_G1 = 256 << 10;
constexpr int G1_STRIDE = 13 * 128;
constexpr int WS_G2 = 768 << 10;
constexpr int WS_END = WS_G2 + 20 * 32768;
constexpr int PSTR = 208;

__device__ __forceinline__ rsrc_t mk_rsrc(const void* p, int bytes) { return __builtin_amdgcn_make_buffer_rsrc((void*)p, 0, bytes, 0x00020000); }
__device__ __forceinline__ u64 up2m(u64 p) { return (p + 0x1fffff) & ~0x1fffffull; }
__device__ __forceinline__ float asf(unsigned u) { return __builtin_bit_cast(float, u); }
__device__ __forceinline__ unsigned asu(float f) { return __builtin_bit_cast(unsigned, f); }
template <int CTRL> __device__ __forceinline__ float dpp(float x) { return asf(__builtin_amdgcn_mov_dpp(asu(x), CTRL, 0xf, 0xf, true)); }
constexpr int XOR1 = 0xB1, XOR2 = 0x4E, HMIR = 0x141, ROR8 = 0x128;
__device__ __forceinline__ unsigned pk_bf16(float lo, float hi) { bf16x2 r = {(__bf16)lo, (__bf16)hi}; return __builtin_bit_cast(unsigned, r); }
__device__ __forceinline__ float dot2(unsigned a, unsigned b, float c) { return __builtin_amdgcn_fdot2_f32_bf16(__builtin_bit_cast(bf16x2, a), __builtin_bit_cast(bf16x2, b), c, false); }
__device__ __forceinline__ f4 mfma(v4u a, v4u b, f4 c) { return __builtin_amdgcn_mfma_f32_16x16x32_bf16(__builtin_bit_cast(bf16x8, a), __builtin_bit_cast(bf16x8, b), c, 0, 0, 0); }
__device__ __forceinline__ float sum8(float x) { x += dpp<XOR1>(x); x += dpp<XOR2>(x); x += dpp<HMIR>(x); return x; }
__device__ __forceinline__ float max8(float x) { x = fmaxf(x, dpp<XOR1>(x)); x = fmaxf(x, dpp<XOR2>(x)); x = fmaxf(x, dpp<HMIR>(x)); return x; }
__device__ __forceinline__ void swap32(float& a, float& b) { auto r = __builtin_amdgcn_permlane32_swap(asu(a), asu(b), false, false); a = asf(r[0]); b = asf(r[1]); }
__device__ __forceinline__ void swap16(float& a, float& b) { auto r = __builtin_amdgcn_permlane16_swap(asu(a), asu(b), false, false); a = asf(r[0]); b = asf(r[1]); }
__device__ __forceinline__ float sum_hi(float x) { x += dpp<ROR8>(x); float y = x; swap16(x, y); x += y; y = x; swap32(x, y); return x + y; }
__device__ __forceinline__ float max_hi(float x) { x = fmaxf(x, dpp<ROR8>(x)); float y = x; swap16(x, y); x = fmaxf(x, y); y = x; swap32(x, y); return fmaxf(x, y); }
__device__ __forceinline__ unsigned miss(v4u g, unsigned tag) { return (g[1] ^ tag) | (g[3] ^ tag); }
__device__ __forceinline__ bool none(unsigned x) { return __builtin_amdgcn_ballot_w64(x != 0) == 0; }
__device__ __forceinline__ unsigned tag_of(unsigned epoch) { return __builtin_amdgcn_readfirstlane(epoch) * 0x9E3779B1u + 0x7F4A7C15u; }
__device__ __forceinline__ v4u pos_mask(int c7) {
  v4u m;
#pragma unroll
  for (int d = 0; d < 4; ++d) m[d] = ((c7 >> 1) == d) ? ((c7 & 1) ? 0xffff0000u : 0x0000ffffu) : 0u;
  return m;
}
__device__ __forceinline__ void fold_tile(f4 d, float& o0, float& o1) {
  float a = d[0], b = d[2], c = d[1], e = d[3];
  swap32(a, b); swap32(c, e);
  o0 = a + dpp<ROR8>(b); o1 = c + dpp<ROR8>(e);
}

#define SB() __builtin_amdgcn_sched_barrier(0)
#define BARRIER() do { SB(); asm volatile("s_waitcnt lgkmcnt(0)\n\ts_barrier" ::: "memory"); SB(); } while (0)
#define PINV(x) asm volatile("" : "+v"(x))
#define PINS(x) asm volatile("" : "+s"(x))
#define POLL _Pragma("nounroll") for (unsigned spin = 0; spin < SPIN_CAP; ++spin)
#define AGAIN() asm volatile("" ::: "memory")
#define V64_77 "v64", "v65", "v66", "v67", "v68", "v69", "v70", "v71", "v72", "v73", "v74", "v75", "v76", "v77"
#define V78_83 "v78", "v79", "v80", "v81", "v82", "v83"
#define V84_103 "v84", "v85", "v86", "v87", "v88", "v89", "v90", "v91", "v92", "v93", "v94", "v95", "v96", "v97", "v98", "v99", "v100", "v101", "v102", "v103"

__global__ void __launch_bounds__(NW * 64, 4) fused_decode(const u16*, const u16*, const u16*, const u16*, const u16*, u16*, unsigned char*, const void*) {
  __shared__ __attribute__((aligned(16))) float lds[16 * PSTR + 96 + 64 + 8 + 8 + 4];
  LDSP float* P = (LDSP float*)lds;
  LDSP unsigned* qs = (LDSP unsigned*)(P + 16 * PSTR);
  LDSP unsigned* os = qs + 96;
  LDSP float* ms = P + 16 * PSTR + 96 + 64;
  LDSP float* ls = ms + 8;
  LDSP unsigned* ctl = (LDSP unsigned*)(ls + 8);
  asm volatile("s_load_dwordx16 s[64:79], %0, 0x0" :: "s"(__builtin_amdgcn_kernarg_segment_ptr())
               : "s64", "s65", "s66", "s67", "s68", "s69", "s70", "s71", "s72", "s73", "s74", "s75", "s76", "s77", "s78", "s79");
#define ARG3(p0, i0, p1, i1, p2, i2) u64 p0, p1, p2; \
  asm volatile("s_waitcnt lgkmcnt(0)\n\ts_mov_b64 %0, s[%c3:%c4]\n\ts_mov_b64 %1, s[%c5:%c6]\n\ts_mov_b64 %2, s[%c7:%c8]" : "=s"(p0), "=s"(p1), "=s"(p2) \
               : "i"(64 + 2 * i0), "i"(65 + 2 * i0), "i"(64 + 2 * i1), "i"(65 + 2 * i1), "i"(64 + 2 * i2), "i"(65 + 2 * i2))
  unsigned pf = 0;
  if (threadIdx.x < 128) {
    u64 pc; asm volatile("s_getpc_b64 %0" : "=s"(pc));
    pf = __builtin_amdgcn_raw_buffer_load_b32(mk_rsrc((void*)(pc & ~127ull), CODE_BYTES), threadIdx.x * 128 + 128, 0, 0);
  }
  SB();
  const int b = blockIdx.x, jb = b >> 3;
  const int h = (b & 7) * 4 + (jb >> 3), s = jb & 7;
  const int tid = threadIdx.x, wave = __builtin_amdgcn_readfirstlane(tid >> 6), lane = tid & 63, r8 = lane >> 3, c7 = lane & 7;
#define SIDE(c) (((sides >> (c)) & 1) << 13)

  if (wave >= 3 && wave < 8) {
    const int rq = s & 3, ch = s >> 2;
    ARG3(wo, 4, out, 5, ws0, 6);
    const u64 ws = up2m(ws0);
    const rsrc_t rws = mk_rsrc((void*)ws, WS_END), rout = mk_rsrc((void*)out, D * 2);
    unsigned ep = 0;
    if (b < 8) {
      __builtin_amdgcn_raw_buffer_store_b128(v4u{0u, 0u, 0u, 0u}, rws, lane < 16 ? r8 * 8192 + c7 * 16 : OFF, WS_PR + b * 1024 + wave * 128, AUX_SC1);
      ep = __builtin_amdgcn_raw_buffer_load_b32(rws, 0, WS_EPOCH, AUX_SC1);
    }
    BARRIER();
    const rsrc_t ro = mk_rsrc((const u16*)wo + (size_t)(h * DH) * D + 320 * ch + 64 * (wave - 3), DH * D * 2);
    v4u msk = pos_mask(c7);
    int wl = (8 * (r8 & 3) + (r8 >> 2)) * (D * 2) + c7 * 16;
    int wsf[3];
#pragma unroll
    for (int i = 0; i < 3; ++i) { const int n = 3 * rq + i; wsf[i] = (32 * (n >> 1) + 2 * (n & 1)) * (D * 2); PINS(wsf[i]); }
    const int yl = 4 * (wave - 3) + (lane >> 4), yp = h * 4 + rq;
    int ys = WS_G2 + (yp >> 6) * 16384 + (yp & 63) * 128;
    const int lc = (b & 7) * 5 + (jb >> 2), qd = jb & 3;
    const bool fin = wave == 4 && jb < 20;
    int fo = (lane >> 1) * 128 + (lane & 1) * 16;
    int oo = (lane & 30) ? OFF : 2 * (64 * (lc >> 2) + 8 * (2 * qd + (lane & 1)) + 4 * (lc & 1) + (lc & 2) + (lane >> 5));
    const LDSP unsigned* op = os + 8 * r8;
    PINV(fo); PINV(oo); PINV(wl); PINV(msk); PINV(op); PINS(ys);
    if (b < 8) {
      asm volatile("s_waitcnt vmcnt(0)" ::: "memory");
      const int first = (wave ^ b) & 1, o0 = WS_PR + b * 1024 + wave * 128 + first * 8192, o1 = o0 ^ 8192;
      u64 c0, c1, c2; unsigned unused;
      asm volatile("s_memtime %1\n\ts_waitcnt lgkmcnt(0)\n\ts_load_dword %0, %4, %5\n\ts_waitcnt lgkmcnt(0)\n\ts_memtime %2\n\ts_waitcnt lgkmcnt(0)\n\t"
                   "s_load_dword %0, %4, %6\n\ts_waitcnt lgkmcnt(0)\n\ts_memtime %3\n\ts_waitcnt lgkmcnt(0)"
                   : "=&s"(unused), "=&s"(c0), "=&s"(c1), "=&s"(c2) : "s"(ws), "s"(o0), "s"(o1) : "memory");
      const int hi = (__builtin_amdgcn_s_getreg(20 | (3 << 11)) >> 2) & 1;
      int d = max(-1000, min(1000, (int)(c1 - c0) - (int)(c2 - c1)));
      if (first ^ hi) d = -d;
      __builtin_amdgcn_raw_buffer_store_b64(v2u{(unsigned)(2 * d + hi), tag_of(ep)}, rws, lane ? OFF : b * 128 + wave * 8, WS_SD, AUX_SC1);
    }
    BARRIER();
    const unsigned tag = __builtin_amdgcn_readfirstlane(ctl[0]);
    if (wave == 7) {
      v2u sd;
      POLL {
        sd = __builtin_amdgcn_raw_buffer_load_b64(rws, r8 * 128 + max(c7, 3) * 8, WS_SD, AUX_SC1);
        if (none(sd[1] ^ tag)) break;
        AGAIN();
      }
      const float dd = sum_hi(sum8(c7 >= 3 ? (float)((int)sd[0] >> 1) : 0.f));
      const u64 bl = __builtin_amdgcn_ballot_w64(((sd[0] & 1) != 0) != (dd > 0.f));
      const unsigned sb = (bl & 1) | ((bl >> 7) & 2) | ((bl >> 14) & 4) | ((bl >> 21) & 8);
      if (lane == 0) ctl[3] = sb | ((sb ^ 15u) << 4);
    }
    BARRIER();
    const unsigned sides = __builtin_amdgcn_readfirstlane(ctl[3]);
    int fs[4];
#pragma unroll
    for (int i = 0; i < 4; ++i) { fs[i] = WS_G2 + (lc % 20) * 32768 + (i >> 1) * 16384 + SIDE(b & 7) + (i & 1) * 4096 + qd * 32; PINS(fs[i]); }
    int yo = (lane & 8) ? OFF : yl * 32768 + SIDE(4 * ch + yl / 5) + c7 * 16;
    PINV(yo);
    if (fin) {
      v4u g[4];
#pragma unroll
      for (int i = 0; i < 4; ++i) g[i] = __builtin_amdgcn_raw_buffer_load_b128(rws, fo, fs[i], AUX_SC1);
      asm volatile("" :: "v"(g[0]), "v"(g[1]), "v"(g[2]), "v"(g[3]));
    }
    BARRIER();
    v4u wr[6];
#pragma unroll
    for (int j = 0; j < 6; ++j) wr[j] = __builtin_amdgcn_raw_buffer_load_b128(ro, wl, wsf[j >> 1] + (j & 1) * 4 * (D * 2), AUX_NT);
    BARRIER();
    BARRIER();
    asm volatile(
        "v_mov_b32 v101, %[tg]\n\tv_mov_b32 v103, %[tg]\n\t"
        "s_barrier\n\t"
        "ds_read_b128 v[64:67], %[op]\n\tds_read_b64 v[68:69], %[op] offset:16\n\t"
        "s_waitcnt lgkmcnt(1)\n\t"
        "v_and_b32 v72, v64, %[m0]\n\tv_and_b32 v73, v64, %[m1]\n\tv_and_b32 v74, v64, %[m2]\n\tv_and_b32 v75, v64, %[m3]\n\t"
        "v_and_b32 v76, v65, %[m0]\n\tv_and_b32 v77, v65, %[m1]\n\t"
        "v_mfma_f32_16x16x32_bf16 v[96:99], v[72:75], %[w0], 0\n\t"
        "v_and_b32 v78, v65, %[m2]\n\tv_and_b32 v79, v65, %[m3]\n\tv_and_b32 v80, v66, %[m0]\n\tv_and_b32 v81, v66, %[m1]\n\t"
        "v_mfma_f32_16x16x32_bf16 v[96:99], v[76:79], %[w1], v[96:99]\n\t"
        "v_and_b32 v82, v66, %[m2]\n\tv_and_b32 v83, v66, %[m3]\n\tv_and_b32 v84, v67, %[m0]\n\tv_and_b32 v85, v67, %[m1]\n\t"
        "v_mfma_f32_16x16x32_bf16 v[96:99], v[80:83], %[w2], v[96:99]\n\t"
        "v_and_b32 v86, v67, %[m2]\n\tv_and_b32 v87, v67, %[m3]\n\ts_waitcnt lgkmcnt(0)\n\tv_and_b32 v88, v68, %[m0]\n\tv_and_b32 v89, v68, %[m1]\n\t"
        "v_mfma_f32_16x16x32_bf16 v[96:99], v[84:87], %[w3], v[96:99]\n\t"
        "v_and_b32 v90, v68, %[m2]\n\tv_and_b32 v91, v68, %[m3]\n\tv_and_b32 v92, v69, %[m0]\n\tv_and_b32 v93, v69, %[m1]\n\t"
        "v_mfma_f32_16x16x32_bf16 v[96:99], v[88:91], %[w4], v[96:99]\n\t"
        "v_and_b32 v94, v69, %[m2]\n\tv_and_b32 v95, v69, %[m3]\n\ts_nop 1\n\t"
        "v_mfma_f32_16x16x32_bf16 v[96:99], v[92:95], %[w5], v[96:99]\n\t"
        "s_nop 7\n\t"
        "v_permlane32_swap_b32 v96, v98\n\tv_permlane32_swap_b32 v97, v99\n\ts_nop 0\n\t"
        "v_add_f32_dpp v100, v98, v96 row_ror:8 row_mask:0xf bank_mask:0xf\n\tv_add_f32_dpp v102, v99, v97 row_ror:8 row_mask:0xf bank_mask:0xf\n\t"
        "buffer_store_dwordx4 v[100:103], %[yo], %[rs], %[ys] offen sc1\n\ts_nop 1"
        :: [tg] "s"(tag), [op] "v"(op), [m0] "v"(msk[0]), [m1] "v"(msk[1]), [m2] "v"(msk[2]), [m3] "v"(msk[3]),
           [w0] "v"(wr[0]), [w1] "v"(wr[1]), [w2] "v"(wr[2]), [w3] "v"(wr[3]), [w4] "v"(wr[4]), [w5] "v"(wr[5]), [yo] "v"(yo), [rs] "s"(rws), [ys] "s"(ys)
        : "memory", V64_77, V78_83, V84_103);
    if (fin) {
      __builtin_amdgcn_s_sleep(H2DLY);
      unsigned cnt = SPIN_CAP;
      asm volatile(
          "1:\n\ts_nop 2\n\t"
          "buffer_load_dwordx4 v[64:67], %[fo], %[rs], %[f0] offen sc1\n\tbuffer_load_dwordx4 v[68:71], %[fo], %[rs], %[f1] offen sc1\n\t"
          "buffer_load_dwordx4 v[72:75], %[fo], %[rs], %[f2] offen sc1\n\tbuffer_load_dwordx4 v[76:79], %[fo], %[rs], %[f3] offen sc1\n\t"
          "s_sub_u32 %[cnt], %[cnt], 1\n\t"
          "s_waitcnt vmcnt(3)\n\tv_bitop3_b32 v65, v65, v67, %[tg] bitop3:0x7e\n\t"
          "s_waitcnt vmcnt(2)\n\tv_bitop3_b32 v69, v69, v71, %[tg] bitop3:0x7e\n\tv_add_f32 v80, v64, v68\n\tv_add_f32 v81, v66, v70\n\t"
          "s_waitcnt vmcnt(1)\n\tv_bitop3_b32 v73, v73, v75, %[tg] bitop3:0x7e\n\tv_or3_b32 v65, v65, v69, v73\n\t"
          "s_waitcnt vmcnt(0)\n\tv_bitop3_b32 v77, v77, v79, %[tg] bitop3:0x7e\n\tv_add_f32 v72, v72, v76\n\tv_add_f32 v74, v74, v78\n\t"
          "v_or_b32 v65, v65, v77\n\tv_add_f32 v80, v80, v72\n\tv_add_f32 v81, v81, v74\n\t"
          "v_cmpx_ne_u32 vcc, 0, v65\n\t"
          "s_cbranch_scc1 2f\n\ts_cbranch_execnz 1b\n"
          "2:\n\ts_mov_b64 exec, -1\n\ts_nop 1\n\t"
          "v_permlane32_swap_b32 v80, v81\n\tv_add_f32 v82, v80, v81\n\tv_add_f32 v83, v80, v81\n\ts_nop 1\n\t"
          "v_permlane16_swap_b32 v82, v83\n\tv_add_f32 v80, v82, v83\n\ts_nop 1\n\t"
          "v_add_f32_dpp v80, v80, v80 row_ror:8 row_mask:0xf bank_mask:0xf\n\ts_nop 1\n\t"
          "v_add_f32_dpp v80, v80, v80 row_ror:4 row_mask:0xf bank_mask:0xf\n\ts_nop 1\n\t"
          "v_add_f32_dpp v80, v80, v80 quad_perm:[2,3,0,1] row_mask:0xf bank_mask:0xf\n\t"
          "v_cvt_pk_bf16_f32 v80, v80, v80\n\t"
          "buffer_store_short v80, %[oo], %[ro], 0 offen"
          : [cnt] "+s"(cnt)
          : [fo] "v"(fo), [rs] "s"(rws), [f0] "s"(fs[0]), [f1] "s"(fs[1]), [f2] "s"(fs[2]), [f3] "s"(fs[3]), [tg] "s"(tag), [oo] "v"(oo), [ro] "s"(rout)
          : "memory", "vcc", "scc", V64_77, V78_83);
    }
    __builtin_amdgcn_endpgm();
  }

  if (wave < 3) {
    const int u = 3 * s + wave;
    int wl = r8 * 10 * (H * DH * 2) + c7 * 16, xl = 20 * r8 + (u & 7) * 160;
    unsigned us = (u & 7) * 80 * (H * DH * 2) + (u >> 3) * 128 + h * (DH * 2);
    PINV(wl); PINV(xl); PINS(us);
    ARG3(wq, 3, x, 0, ws0, 6);
    const rsrc_t rq = mk_rsrc((const char*)wq + us, 79 * H * DH * 2 + 128), rx = mk_rsrc((void*)x, D * 2), rws = mk_rsrc((void*)up2m(ws0), WS_END);
    v4u wr[10], g[5];
    const unsigned start = (unsigned)__builtin_amdgcn_s_memrealtime();
#pragma unroll
    for (int j = 0; j < 10; ++j) wr[j] = __builtin_amdgcn_raw_buffer_load_b128(rq, wl, j * (H * DH * 2), AUX_NT);
    const v4u xa = __builtin_amdgcn_raw_buffer_load_b128(rx, xl, 0, 0);
    const unsigned xb = __builtin_amdgcn_raw_buffer_load_b32(rx, xl + 16, 0, 0);
    const unsigned ep = __builtin_amdgcn_raw_buffer_load_b32(rws, wave ? OFF : 0, WS_EPOCH, AUX_SC1);
    if (tid == 0) ctl[2] = 0;
    BARRIER();
    const v4u msk = pos_mask(c7);
    f4 acc[3];
    acc[0] = f4{0.f, 0.f, 0.f, 0.f};
#pragma unroll
    for (int j = 0; j < 10; ++j) {
      const unsigned xw = j < 8 ? xa[j >> 1] : xb;
      acc[0] = mfma(msk & __builtin_amdgcn_perm(xw, xw, (j & 1) ? 0x03020302u : 0x01000100u), wr[j], acc[0]);
    }
    asm volatile("" :: "v"(pf));
    if (wave) {
      *(LDSP f4*)(P + (wave - 1) * 256 + lane * 4) = acc[0];
      asm volatile("s_waitcnt lgkmcnt(0)" ::: "memory");
      if (lane == 0) __hip_atomic_fetch_add(ctl + 2, 1u, __ATOMIC_RELAXED, __HIP_MEMORY_SCOPE_WORKGROUP);
      if (wave == 2 || b != 0) __builtin_amdgcn_endpgm();
      BARRIER();
      const unsigned tag = __builtin_amdgcn_readfirstlane(ctl[0]);
      BARRIER(); BARRIER();
      const v4u ml = __builtin_amdgcn_raw_buffer_load_b128(rws, (lane & 31) * 1024, WS_ML, AUX_SC1);
      BARRIER(); BARRIER();
      const bool all = none(miss(ml, tag));
      if (!all) {
        BARRIER();
        POLL {
          if (none(miss(__builtin_amdgcn_raw_buffer_load_b128(rws, (lane & 31) * 1024, WS_ML, AUX_SC1), tag))) break;
          AGAIN();
        }
      }
      __builtin_amdgcn_raw_buffer_store_b32(tag, rws, lane ? OFF : 0, WS_EPOCH, AUX_SC1);
      if (all) BARRIER();
      __builtin_amdgcn_endpgm();
    }
    while (__hip_atomic_load(ctl + 2, __ATOMIC_RELAXED, __HIP_MEMORY_SCOPE_WORKGROUP) != 2u) __builtin_amdgcn_s_sleep(1);
    AGAIN();
    acc[1] = *(const LDSP f4*)(P + lane * 4); acc[2] = *(const LDSP f4*)(P + 256 + lane * 4);
    const unsigned tag = tag_of(ep);
    int gqs = WS_GQ + h * GQ_STRIDE;
    PINS(gqs);
    const int rec = s + (s > 2) + (s > 5);
    const int to = (lane & 8) ? OFF : (lane >> 4) * 128 + c7 * 16;
    float q0, q1;
    if (s == 2 || s == 5) {
      fold_tile(s == 2 ? acc[2] : acc[1] + acc[2], q0, q1);
      __builtin_amdgcn_raw_buffer_store_b128(v4u{asu(q0), tag, asu(q1), tag}, rws, to, gqs + (rec + 1) * 512, AUX_SC1);
      fold_tile(s == 2 ? acc[0] + acc[1] : acc[0], q0, q1);
    } else fold_tile((acc[0] + acc[1]) + acc[2], q0, q1);
    __builtin_amdgcn_raw_buffer_store_b128(v4u{asu(q0), tag, asu(q1), tag}, rws, to, gqs + rec * 512, AUX_SC1);
    SB();
    __builtin_amdgcn_s_sleep(QDLY);
    while ((unsigned)__builtin_amdgcn_s_memrealtime() - start < QT) __builtin_amdgcn_s_sleep(2);
    POLL {
#pragma unroll
      for (int i = 0; i < 5; ++i) g[i] = __builtin_amdgcn_raw_buffer_load_b128(rws, lane * 16, gqs + i * 1024, AUX_SC1);
      if (none(miss(g[0], tag) | miss(g[1], tag) | miss(g[2], tag) | miss(g[3], tag) | miss(g[4], tag))) break;
      AGAIN();
    }
    {
      const bool hi = lane >= 32;
      float qa[3][2];
#pragma unroll
      for (int e = 0; e < 2; ++e) {
        qa[0][e] = asf(g[0][2 * e]) + (hi ? 0.f : asf(g[1][2 * e]));
        qa[1][e] = asf(g[2][2 * e]) + (hi ? asf(g[1][2 * e]) : asf(g[3][2 * e]));
        qa[2][e] = asf(g[4][2 * e]) + (hi ? asf(g[3][2 * e]) : 0.f);
      }
#pragma unroll
      for (int t = 0; t < 3; ++t) {
        float a0 = qa[t][0], a1 = qa[t][1], b0 = a0, b1 = a1;
        swap32(a0, b0); swap32(a1, b1);
        qs[32 * t + 4 * c7 + 2 * (r8 & 1) + ((r8 >> 1) & 1)] = pk_bf16(a0 + b0, a1 + b1);
      }
    }
    if (lane == 0) ctl[0] = tag;
    BARRIER();
    BARRIER();
    const unsigned sides = __builtin_amdgcn_readfirstlane(ctl[3]);
    int g1o = (r8 >> 2) * 16384 + (r8 & 3) * G1_STRIDE + c7 * 16, g1q = WS_G1 + (h & 15) * 32768 + SIDE(b & 7) + 3 * (s & 3) * 128;
    int mls = WS_ML + h * 1024;
    PINS(mls); PINS(g1q); PINV(g1o);
    const float mw = ms[c7], M = max8(mw);
    const float L = sum8(ls[c7] * __builtin_amdgcn_exp2f(mw - M));
    __builtin_amdgcn_raw_buffer_store_b128(v4u{asu(M), tag, asu(L), tag}, rws, lane < 8 ? lane * 16 : OFF, mls + s * 128, AUX_SC1);
    SB();
#pragma unroll
    for (int i = 0; i < 3; ++i) g[i] = __builtin_amdgcn_raw_buffer_load_b128(rws, g1o + i * 128, g1q, AUX_SC1);
    SB();
    __builtin_amdgcn_s_sleep(MDLY);
    asm volatile("" :: "v"(g[0]), "v"(g[1]), "v"(g[2]));
    POLL {
      g[3] = __builtin_amdgcn_raw_buffer_load_b128(rws, lane * 16, mls, AUX_SC1);
      if (none(miss(g[3], tag))) break;
      AGAIN();
    }
    const float Ms = asf(g[3][0]), Mg = max_hi(Ms);
    const float Lg = sum_hi(__builtin_amdgcn_exp2f(Ms - Mg) * asf(g[3][2]));
    if (lane == 0) ctl[1] = asu(__builtin_amdgcn_exp2f(M - Mg) / Lg);
    LDSP unsigned* ow0 = os + 8 * c7 + (lane >> 4); LDSP unsigned* ow1 = os + 8 * c7 + 4 + (lane >> 5);
    PINV(ow0); PINV(ow1);
    BARRIER();
    BARRIER();
    BARRIER();
    __builtin_amdgcn_s_sleep(H1DLY);
    unsigned cnt = SPIN_CAP;
    asm volatile(
        "1:\n\t"
        "buffer_load_dwordx4 v[64:67], %[go], %[rs], %[gq] offen sc1\n\tbuffer_load_dwordx4 v[68:71], %[go], %[rs], %[gq] offen offset:128 sc1\n\t"
        "buffer_load_dwordx4 v[72:75], %[go], %[rs], %[gq] offen offset:256 sc1\n\t"
        "s_sub_u32 %[cnt], %[cnt], 1\n\t"
        "s_waitcnt vmcnt(2)\n\tv_bitop3_b32 v65, v65, v67, %[tg] bitop3:0x7e\n\t"
        "s_waitcnt vmcnt(1)\n\tv_bitop3_b32 v69, v69, v71, %[tg] bitop3:0x7e\n\t"
        "v_permlane32_swap_b32 v64, v68\n\tv_permlane32_swap_b32 v66, v70\n\tv_add_f32 v64, v64, v68\n\tv_add_f32 v66, v66, v70\n\t"
        "s_waitcnt vmcnt(0)\n\tv_permlane32_swap_b32 v72, v74\n\tv_bitop3_b32 v73, v73, v75, %[tg] bitop3:0x7e\n\t"
        "v_add_f32 v76, v72, v74\n\tv_add_f32 v77, v72, v74\n\tv_or3_b32 v65, v65, v69, v73\n\t"
        "v_permlane16_swap_b32 v64, v66\n\tv_cmp_ne_u32 vcc, 0, v65\n\tv_permlane16_swap_b32 v76, v77\n\t"
        "v_add_f32 v64, v64, v66\n\tv_add_f32 v76, v76, v77\n\t"
        "s_cbranch_scc1 2f\n\ts_cbranch_vccnz 1b\n"
        "2:\n\ts_nop 0\n\t"
        "v_add_f32_dpp v64, v64, v64 row_ror:8 row_mask:0xf bank_mask:0xf\n\tv_add_f32_dpp v76, v76, v76 row_ror:8 row_mask:0xf bank_mask:0xf\n\t"
        "v_cvt_pk_bf16_f32 v64, v64, v64\n\tv_cvt_pk_bf16_f32 v76, v76, v76\n\t"
        "ds_write_b32 %[o0], v64\n\tds_write_b32 %[o1], v76"
        : [cnt] "+s"(cnt)
        : [go] "v"(g1o), [rs] "s"(rws), [gq] "s"(g1q), [tg] "s"(tag), [o0] "v"(ow0), [o1] "v"(ow1)
        : "memory", "vcc", "scc", V64_77);
    BARRIER();
    __builtin_amdgcn_endpgm();
  }

  {
    const int w = wave - 8;
    unsigned kvo = (unsigned)(h * SK + (SK - W) + (w * 8 + ((s + w) & 7)) * 8) * (DH * 2);
    int lo = r8 * (DH * 2) + c7 * 16;
    PINV(lo); PINS(kvo);
    ARG3(kc, 1, vc, 2, ws0, 6);
    const rsrc_t rk = mk_rsrc((const char*)kc + kvo, (7 * 512 + 8) * DH * 2), rv = mk_rsrc((const char*)vc + kvo, (7 * 512 + 8) * DH * 2);
    v4u kv[48];
    const unsigned start = (unsigned)__builtin_amdgcn_s_memrealtime();
#define ISSUE(n) kv[n] = __builtin_amdgcn_raw_buffer_load_b128((n) < 24 ? rk : rv, lo + ((n) % 3) * 128, (((n) % 24) / 3) * 512 * DH * 2, AUX_NT)
    int late = 0;
#define STEP(n) do { SB(); \
      if ((n) <= N_AD) asm volatile("s_waitcnt vmcnt(%0)" :: "n"(WIN - 1) : "memory"); \
      else asm volatile("s_cmp_gt_i32 %0, 0\n\ts_waitcnt vmcnt(%1)\n\ts_cbranch_scc1 1f\n\ts_waitcnt vmcnt(%2)\n1:" :: "s"(late), "n"(WIDE - 1), "n"(WIN - 1) : "memory", "scc"); \
      ISSUE(n); SB(); \
      if ((n) >= N_AD && (n) % 3 == 2 && (n) < 47) { late = (int)((unsigned)__builtin_amdgcn_s_memrealtime() - start) - (T0 + (n) * DT / 10); PINS(late); SB(); } } while (0)
#pragma unroll
    for (int n = 0; n < WIN; ++n) ISSUE(n);
    BARRIER();
#pragma unroll
    for (int n = WIN; n <= N_QB; ++n) STEP(n);
    BARRIER();
    v4u qv[3];
#pragma unroll
    for (int t = 0; t < 3; ++t) qv[t] = *(const LDSP v4u*)(qs + 32 * t + 4 * c7);
    const unsigned tag = ctl[0];
    constexpr float C2 = 0.07216878364870322f * 1.4426950408889634f;
    constexpr int N_SC = (N_QB + 7 > 23 + WIDE) ? N_QB + 7 : 23 + WIDE;
    float sc[NG];
#pragma unroll
    for (int n = N_QB; n <= N_SC; ++n) {
      if (n > N_QB) STEP(n);
#pragma unroll
      for (int j = 0; j < NG; ++j) {
        if (n == ((N_QB + j > 3 * j + 2 + WIDE) ? N_QB + j : 3 * j + 2 + WIDE)) {
          float a = 0.f;
#pragma unroll
          for (int t = 0; t < 3; ++t)
#pragma unroll
            for (int k = 0; k < 4; ++k) a = dot2(kv[3 * j + t][k], qv[t][k], a);
          sc[j] = sum8(a) * C2;
          PINV(sc[j]);
        }
      }
    }
    float m = sc[0];
#pragma unroll
    for (int j = 1; j < NG; ++j) m = fmaxf(m, sc[j]);
    m = max_hi(m);
    STEP(N_SC + 1);
    float l = 0.f;
#pragma unroll
    for (int j = 0; j < NG; ++j) { sc[j] = __builtin_amdgcn_exp2f(sc[j] - m); l += sc[j]; }
    l = sum_hi(l);
    if (lane == 0) { ms[w] = m; ls[w] = l; }
    STEP(N_SC + 2);
    BARRIER();
    const float wt = __builtin_amdgcn_exp2f(m - max8(ms[c7]));
    const v4u msk = pos_mask(c7);
    f4 acc[3] = {{0.f, 0.f, 0.f, 0.f}, {0.f, 0.f, 0.f, 0.f}, {0.f, 0.f, 0.f, 0.f}};
    v4u pa;
#define PV(n) do { if ((n) % 3 == 0) { const float p = sc[((n) - 24) / 3] * wt; pa = msk & pk_bf16(p, p); } acc[(n) % 3] = mfma(pa, kv[n], acc[(n) % 3]); } while (0)
#pragma unroll
    for (int n = 24; n <= N_SC + 2 - WIDE; ++n) PV(n);
#pragma unroll
    for (int n = N_SC + 3; n < 48; ++n) { STEP(n); if (n == 47) BARRIER(); PV(n - WIDE); }
    LDSP float* pp = P + (2 * w + (lane >> 5)) * PSTR + 8 * c7 + 4 * ((lane >> 4) & 1);
    const bool pvalid = (lane >> 5) == ((lane >> 3) & 1);
    const LDSP float* rp = P + 2 * r8 * PSTR + 32 * w + 4 * c7;
    const rsrc_t rws = mk_rsrc((void*)up2m(ws0), WS_END);
    const unsigned sides = __builtin_amdgcn_readfirstlane(ctl[3]);
    int so = (lane & 8) ? OFF : (lane >> 4) * 64 + c7 * 8;
    int ss = WS_G1 + (h & 15) * 32768 + SIDE(b & 7) + (s >> 2) * 16384 + (s & 3) * G1_STRIDE + w * 256;
    float f = asf(ctl[1]);
    v2u sv; sv[1] = tag;
    PINV(so); PINS(ss); PINV(f); PINV(pp); PINV(rp); PINV(sv);
#pragma unroll
    for (int n = 48 - WIDE; n < 45; ++n) PV(n);
#pragma unroll
    for (int n = 45; n < 48; ++n) { PV(n); if (pvalid) *(LDSP f4*)(pp + 64 * (n - 45)) = acc[n - 45]; SB(); }
    BARRIER();
    if (w < 6) {
      const f4 t = *(const LDSP f4*)rp + *(const LDSP f4*)(rp + PSTR);
      float t0 = t[0], t1 = t[1], t2 = t[2], t3 = t[3];
      swap32(t0, t2); t0 += t2;
      swap32(t1, t3); t1 += t3;
      swap16(t0, t1); t0 += t1;
      t0 += dpp<ROR8>(t0);
      sv[0] = asu(t0 * f);
      __builtin_amdgcn_raw_buffer_store_b64(sv, rws, so, ss, AUX_SC1);
    }
    BARRIER();
    __builtin_amdgcn_endpgm();
  }
}

extern "C" void kernel_launch(void* const* d_in, const int* in_sizes, int n_in, void* d_out, int out_size, void* d_ws, size_t ws_size, hipStream_t stream) {
  fused_decode<<<256, NW * 64, 0, stream>>>((const u16*)d_in[0], (const u16*)d_in[1], (const u16*)d_in[2], (const u16*)d_in[3], (const u16*)d_in[4],
                                            (u16*)d_out, (unsigned char*)d_ws, d_ws);
}
```

```cpp
#include <hip/hip_runtime.h>
#include <hip/hip_bf16.h>
#include <stdint.h>

typedef unsigned short u16;
typedef unsigned long long u64;
typedef unsigned v4u __attribute__((ext_vector_type(4)));
typedef unsigned v2u __attribute__((ext_vector_type(2)));
typedef float f4 __attribute__((ext_vector_type(4)));
typedef __bf16 bf16x2 __attribute__((ext_vector_type(2)));
typedef __bf16 bf16x8 __attribute__((ext_vector_type(8)));
typedef __amdgpu_buffer_rsrc_t rsrc_t;
#define LDSP __attribute__((address_space(3)))

constexpr int D = 640, H = 32, SK = 32768, DH = 192, W = 4096;
constexpr int NW = 16, NG = 8;
constexpr int WIN = 3;
constexpr int WIDE = 5;
constexpr int T0 = 110, DT = 292, N_AD = 8;
constexpr int N_QB = 20;
constexpr int QDLY = 12, MDLY = 70, H1DLY = 8, H2DLY = 13;
constexpr unsigned QT = 500;
constexpr int CODE_BYTES = 9728;
constexpr unsigned SPIN_CAP = 1u << 20;
constexpr int AUX_NT = 2, AUX_SC1 = 16;
constexpr int OFF = 0x7ffffff0;

constexpr int WS_EPOCH = 0;
constexpr int WS_SD = 1024;
constexpr int WS_GQ = 4096;
constexpr int GQ_STRIDE = 10 * 512;
constexpr int WS_ML = WS_GQ + 32 * GQ_STRIDE;
constexpr int WS_PR = 224 << 10;
constexpr int WS_G1 = 256 << 10;
constexpr int G1_STRIDE = 13 * 128;
constexpr int WS_G2 = 768 << 10;
constexpr int WS_END = WS_G2 + 20 * 32768;
constexpr int PSTR = 208;

__device__ __forceinline__ rsrc_t mk_rsrc(const void* p, int bytes) { return __builtin_amdgcn_make_buffer_rsrc((void*)p, 0, bytes, 0x00020000); }
__device__ __forceinline__ u64 up2m(u64 p) { return (p + 0x1fffff) & ~0x1fffffull; }
__device__ __forceinline__ float asf(unsigned u) { return __builtin_bit_cast(float, u); }
__device__ __forceinline__ unsigned asu(float f) { return __builtin_bit_cast(unsigned, f); }
template <int CTRL> __device__ __forceinline__ float dpp(float x) { return asf(__builtin_amdgcn_mov_dpp(asu(x), CTRL, 0xf, 0xf, true)); }
constexpr int XOR1 = 0xB1, XOR2 = 0x4E, HMIR = 0x141, ROR8 = 0x128;
__device__ __forceinline__ unsigned pk_bf16(float lo, float hi) { bf16x2 r = {(__bf16)lo, (__bf16)hi}; return __builtin_bit_cast(unsigned, r); }
__device__ __forceinline__ float dot2(unsigned a, unsigned b, float c) { return __builtin_amdgcn_fdot2_f32_bf16(__builtin_bit_cast(bf16x2, a), __builtin_bit_cast(bf16x2, b), c, false); }
__device__ __forceinline__ f4 mfma(v4u a, v4u b, f4 c) { return __builtin_amdgcn_mfma_f32_16x16x32_bf16(__builtin_bit_cast(bf16x8, a), __builtin_bit_cast(bf16x8, b), c, 0, 0, 0); }
__device__ __forceinline__ float sum8(float x) { x += dpp<XOR1>(x); x += dpp<XOR2>(x); x += dpp<HMIR>(x); return x; }
__device__ __forceinline__ float max8(float x) { x = fmaxf(x, dpp<XOR1>(x)); x = fmaxf(x, dpp<XOR2>(x)); x = fmaxf(x, dpp<HMIR>(x)); return x; }
__device__ __forceinline__ void swap32(float& a, float& b) { auto r = __builtin_amdgcn_permlane32_swap(asu(a), asu(b), false, false); a = asf(r[0]); b = asf(r[1]); }
__device__ __forceinline__ void swap16(float& a, float& b) { auto r = __builtin_amdgcn_permlane16_swap(asu(a), asu(b), false, false); a = asf(r[0]); b = asf(r[1]); }
__device__ __forceinline__ float sum_hi(float x) { x += dpp<ROR8>(x); float y = x; swap16(x, y); x += y; y = x; swap32(x, y); return x + y; }
__device__ __forceinline__ float max_hi(float x) { x = fmaxf(x, dpp<ROR8>(x)); float y = x; swap16(x, y); x = fmaxf(x, y); y = x; swap32(x, y); return fmaxf(x, y); }
__device__ __forceinline__ unsigned miss(v4u g, unsigned tag) { return (g[1] ^ tag) | (g[3] ^ tag); }
__device__ __forceinline__ bool none(unsigned x) { return __builtin_amdgcn_ballot_w64(x != 0) == 0; }
__device__ __forceinline__ unsigned tag_of(unsigned epoch) { return __builtin_amdgcn_readfirstlane(epoch) * 0x9E3779B1u + 0x7F4A7C15u; }
__device__ __forceinline__ v4u pos_mask(int c7) {
  v4u m;
#pragma unroll
  for (int d = 0; d < 4; ++d) m[d] = ((c7 >> 1) == d) ? ((c7 & 1) ? 0xffff0000u : 0x0000ffffu) : 0u;
  return m;
}
__device__ __forceinline__ void fold_tile(f4 d, float& o0, float& o1) {
  float a = d[0], b = d[2], c = d[1], e = d[3];
  swap32(a, b); swap32(c, e);
  o0 = a + dpp<ROR8>(b); o1 = c + dpp<ROR8>(e);
}

#define SB() __builtin_amdgcn_sched_barrier(0)
#define BARRIER() do { SB(); asm volatile("s_waitcnt lgkmcnt(0)\n\ts_barrier" ::: "memory"); SB(); } while (0)
#define PINV(x) asm volatile("" : "+v"(x))
#define PINS(x) asm volatile("" : "+s"(x))
#define POLL _Pragma("nounroll") for (unsigned spin = 0; spin < SPIN_CAP; ++spin)
#define AGAIN() asm volatile("" ::: "memory")
#define V64_77 "v64", "v65", "v66", "v67", "v68", "v69", "v70", "v71", "v72", "v73", "v74", "v75", "v76", "v77"
#define V78_83 "v78", "v79", "v80", "v81", "v82", "v83"
#define V84_103 "v84", "v85", "v86", "v87", "v88", "v89", "v90", "v91", "v92", "v93", "v94", "v95", "v96", "v97", "v98", "v99", "v100", "v101", "v102", "v103"

__global__ void __launch_bounds__(NW * 64, 4) fused_decode(const u16*, const u16*, const u16*, const u16*, const u16*, u16*, unsigned char*, const void*) {
  __shared__ __attribute__((aligned(16))) float lds[16 * PSTR + 96 + 64 + 8 + 8 + 4];
  LDSP float* P = (LDSP float*)lds;
  LDSP unsigned* qs = (LDSP unsigned*)(P + 16 * PSTR);
  LDSP unsigned* os = qs + 96;
  LDSP float* ms = P + 16 * PSTR + 96 + 64;
  LDSP float* ls = ms + 8;
  LDSP unsigned* ctl = (LDSP unsigned*)(ls + 8);
  u64 t00; asm volatile("s_memrealtime %0\n\ts_waitcnt lgkmcnt(0)" : "=s"(t00) :: "memory"); SB();
  asm volatile("s_load_dwordx16 s[64:79], %0, 0x0" :: "s"(__builtin_amdgcn_kernarg_segment_ptr())
               : "s64", "s65", "s66", "s67", "s68", "s69", "s70", "s71", "s72", "s73", "s74", "s75", "s76", "s77", "s78", "s79");
#define ARG3(p0, i0, p1, i1, p2, i2) u64 p0, p1, p2; \
  asm volatile("s_waitcnt lgkmcnt(0)\n\ts_mov_b64 %0, s[%c3:%c4]\n\ts_mov_b64 %1, s[%c5:%c6]\n\ts_mov_b64 %2, s[%c7:%c8]" : "=s"(p0), "=s"(p1), "=s"(p2) \
               : "i"(64 + 2 * i0), "i"(65 + 2 * i0), "i"(64 + 2 * i1), "i"(65 + 2 * i1), "i"(64 + 2 * i2), "i"(65 + 2 * i2))
  unsigned pf = 0;
  if (threadIdx.x < 128) {
    u64 pc; asm volatile("s_getpc_b64 %0" : "=s"(pc));
    pf = __builtin_amdgcn_raw_buffer_load_b32(mk_rsrc((void*)(pc & ~127ull), CODE_BYTES), threadIdx.x * 128 + 128, 0, 0);
  }
  SB();
  const int b = blockIdx.x, jb = b >> 3;
  const int h = (b & 7) * 4 + (jb >> 3), s = jb & 7;
  const int tid = threadIdx.x, wave = __builtin_amdgcn_readfirstlane(tid >> 6), lane = tid & 63, r8 = lane >> 3, c7 = lane & 7;
#define SIDE(c) (((sides >> (c)) & 1) << 13)

  if (wave >= 3 && wave < 8) {
    const int rq = s & 3, ch = s >> 2;
    ARG3(wo, 4, out, 5, ws0, 6);
    const u64 ws = up2m(ws0);
    const rsrc_t rws = mk_rsrc((void*)ws, WS_END), rout = mk_rsrc((void*)out, D * 2);
    unsigned ep = 0;
    if (b < 8) {
      __builtin_amdgcn_raw_buffer_store_b128(v4u{0u, 0u, 0u, 0u}, rws, lane < 16 ? r8 * 8192 + c7 * 16 : OFF, WS_PR + b * 1024 + wave * 128, AUX_SC1);
      ep = __builtin_amdgcn_raw_buffer_load_b32(rws, 0, WS_EPOCH, AUX_SC1);
    }
    BARRIER();
    const rsrc_t ro = mk_rsrc((const u16*)wo + (size_t)(h * DH) * D + 320 * ch + 64 * (wave - 3), DH * D * 2);
    v4u msk = pos_mask(c7);
    int wl = (8 * (r8 & 3) + (r8 >> 2)) * (D * 2) + c7 * 16;
    int wsf[3];
#pragma unroll
    for (int i = 0; i < 3; ++i) { const int n = 3 * rq + i; wsf[i] = (32 * (n >> 1) + 2 * (n & 1)) * (D * 2); PINS(wsf[i]); }
    const int yl = 4 * (wave - 3) + (lane >> 4), yp = h * 4 + rq;
    int ys = WS_G2 + (yp >> 6) * 16384 + (yp & 63) * 128;
    const int lc = (b & 7) * 5 + (jb >> 2), qd = jb & 3;
    const bool fin = wave == 4 && jb < 20;
    int fo = (lane >> 1) * 128 + (lane & 1) * 16;
    int oo = (lane & 30) ? OFF : 2 * (64 * (lc >> 2) + 8 * (2 * qd + (lane & 1)) + 4 * (lc & 1) + (lc & 2) + (lane >> 5));
    const LDSP unsigned* op = os + 8 * r8;
    PINV(fo); PINV(oo); PINV(wl); PINV(msk); PINV(op); PINS(ys);
    if (b < 8) {
      asm volatile("s_waitcnt vmcnt(0)" ::: "memory");
      const int first = (wave ^ b) & 1, o0 = WS_PR + b * 1024 + wave * 128 + first * 8192, o1 = o0 ^ 8192;
      u64 c0, c1, c2; unsigned unused;
      asm volatile("s_memtime %1\n\ts_waitcnt lgkmcnt(0)\n\ts_load_dword %0, %4, %5\n\ts_waitcnt lgkmcnt(0)\n\ts_memtime %2\n\ts_waitcnt lgkmcnt(0)\n\t"
                   "s_load_dword %0, %4, %6\n\ts_waitcnt lgkmcnt(0)\n\ts_memtime %3\n\ts_waitcnt lgkmcnt(0)"
                   : "=&s"(unused), "=&s"(c0), "=&s"(c1), "=&s"(c2) : "s"(ws), "s"(o0), "s"(o1) : "memory");
      const int hi = (__builtin_amdgcn_s_getreg(20 | (3 << 11)) >> 2) & 1;
      int d = max(-1000, min(1000, (int)(c1 - c0) - (int)(c2 - c1)));
      if (first ^ hi) d = -d;
      __builtin_amdgcn_raw_buffer_store_b64(v2u{(unsigned)(2 * d + hi), tag_of(ep)}, rws, lane ? OFF : b * 128 + wave * 8, WS_SD, AUX_SC1);
    }
    BARRIER();
    const unsigned tag = __builtin_amdgcn_readfirstlane(ctl[0]);
    if (wave == 7) {
      v2u sd;
      POLL {
        sd = __builtin_amdgcn_raw_buffer_load_b64(rws, r8 * 128 + max(c7, 3) * 8, WS_SD, AUX_SC1);
        if (none(sd[1] ^ tag)) break;
        AGAIN();
      }
      const float dd = sum_hi(sum8(c7 >= 3 ? (float)((int)sd[0] >> 1) : 0.f));
      const u64 bl = __builtin_amdgcn_ballot_w64(((sd[0] & 1) != 0) != (dd > 0.f));
      const unsigned sb = (bl & 1) | ((bl >> 7) & 2) | ((bl >> 14) & 4) | ((bl >> 21) & 8);
      if (lane == 0) ctl[3] = sb | ((sb ^ 15u) << 4);
    }
    BARRIER();
    const unsigned sides = __builtin_amdgcn_readfirstlane(ctl[3]);
    int fs[4];
#pragma unroll
    for (int i = 0; i < 4; ++i) { fs[i] = WS_G2 + (lc % 20) * 32768 + (i >> 1) * 16384 + SIDE(b & 7) + (i & 1) * 4096 + qd * 32; PINS(fs[i]); }
    int yo = (lane & 8) ? OFF : yl * 32768 + SIDE(4 * ch + yl / 5) + c7 * 16;
    PINV(yo);
    if (fin) {
      v4u g[4];
#pragma unroll
      for (int i = 0; i < 4; ++i) g[i] = __builtin_amdgcn_raw_buffer_load_b128(rws, fo, fs[i], AUX_SC1);
      asm volatile("" :: "v"(g[0]), "v"(g[1]), "v"(g[2]), "v"(g[3]));
    }
    BARRIER();
    v4u wr[6];
#pragma unroll
    for (int j = 0; j < 6; ++j) wr[j] = __builtin_amdgcn_raw_buffer_load_b128(ro, wl, wsf[j >> 1] + (j & 1) * 4 * (D * 2), AUX_NT);
    BARRIER();
    BARRIER();
    asm volatile(
        "v_mov_b32 v101, %[tg]\n\tv_mov_b32 v103, %[tg]\n\t"
        "s_barrier\n\t"
        "ds_read_b128 v[64:67], %[op]\n\tds_read_b64 v[68:69], %[op] offset:16\n\t"
        "s_waitcnt lgkmcnt(1)\n\t"
        "v_and_b32 v72, v64, %[m0]\n\tv_and_b32 v73, v64, %[m1]\n\tv_and_b32 v74, v64, %[m2]\n\tv_and_b32 v75, v64, %[m3]\n\t"
        "v_and_b32 v76, v65, %[m0]\n\tv_and_b32 v77, v65, %[m1]\n\t"
        "v_mfma_f32_16x16x32_bf16 v[96:99], v[72:75], %[w0], 0\n\t"
        "v_and_b32 v78, v65, %[m2]\n\tv_and_b32 v79, v65, %[m3]\n\tv_and_b32 v80, v66, %[m0]\n\tv_and_b32 v81, v66, %[m1]\n\t"
        "v_mfma_f32_16x16x32_bf16 v[96:99], v[76:79], %[w1], v[96:99]\n\t"
        "v_and_b32 v82, v66, %[m2]\n\tv_and_b32 v83, v66, %[m3]\n\tv_and_b32 v84, v67, %[m0]\n\tv_and_b32 v85, v67, %[m1]\n\t"
        "v_mfma_f32_16x16x32_bf16 v[96:99], v[80:83], %[w2], v[96:99]\n\t"
        "v_and_b32 v86, v67, %[m2]\n\tv_and_b32 v87, v67, %[m3]\n\ts_waitcnt lgkmcnt(0)\n\tv_and_b32 v88, v68, %[m0]\n\tv_and_b32 v89, v68, %[m1]\n\t"
        "v_mfma_f32_16x16x32_bf16 v[96:99], v[84:87], %[w3], v[96:99]\n\t"
        "v_and_b32 v90, v68, %[m2]\n\tv_and_b32 v91, v68, %[m3]\n\tv_and_b32 v92, v69, %[m0]\n\tv_and_b32 v93, v69, %[m1]\n\t"
        "v_mfma_f32_16x16x32_bf16 v[96:99], v[88:91], %[w4], v[96:99]\n\t"
        "v_and_b32 v94, v69, %[m2]\n\tv_and_b32 v95, v69, %[m3]\n\ts_nop 1\n\t"
        "v_mfma_f32_16x16x32_bf16 v[96:99], v[92:95], %[w5], v[96:99]\n\t"
        "s_nop 7\n\t"
        "v_permlane32_swap_b32 v96, v98\n\tv_permlane32_swap_b32 v97, v99\n\ts_nop 0\n\t"
        "v_add_f32_dpp v100, v98, v96 row_ror:8 row_mask:0xf bank_mask:0xf\n\tv_add_f32_dpp v102, v99, v97 row_ror:8 row_mask:0xf bank_mask:0xf\n\t"
        "buffer_store_dwordx4 v[100:103], %[yo], %[rs], %[ys] offen sc1\n\ts_nop 1"
        :: [tg] "s"(tag), [op] "v"(op), [m0] "v"(msk[0]), [m1] "v"(msk[1]), [m2] "v"(msk[2]), [m3] "v"(msk[3]),
           [w0] "v"(wr[0]), [w1] "v"(wr[1]), [w2] "v"(wr[2]), [w3] "v"(wr[3]), [w4] "v"(wr[4]), [w5] "v"(wr[5]), [yo] "v"(yo), [rs] "s"(rws), [ys] "s"(ys)
        : "memory", V64_77, V78_83, V84_103);
    if (fin) {
      __builtin_amdgcn_s_sleep(H2DLY);
      unsigned cnt = SPIN_CAP;
      asm volatile(
          "1:\n\ts_nop 2\n\t"
          "buffer_load_dwordx4 v[64:67], %[fo], %[rs], %[f0] offen sc1\n\tbuffer_load_dwordx4 v[68:71], %[fo], %[rs], %[f1] offen sc1\n\t"
          "buffer_load_dwordx4 v[72:75], %[fo], %[rs], %[f2] offen sc1\n\tbuffer_load_dwordx4 v[76:79], %[fo], %[rs], %[f3] offen sc1\n\t"
          "s_sub_u32 %[cnt], %[cnt], 1\n\t"
          "s_waitcnt vmcnt(3)\n\tv_bitop3_b32 v65, v65, v67, %[tg] bitop3:0x7e\n\t"
          "s_waitcnt vmcnt(2)\n\tv_bitop3_b32 v69, v69, v71, %[tg] bitop3:0x7e\n\tv_add_f32 v80, v64, v68\n\tv_add_f32 v81, v66, v70\n\t"
          "s_waitcnt vmcnt(1)\n\tv_bitop3_b32 v73, v73, v75, %[tg] bitop3:0x7e\n\tv_or3_b32 v65, v65, v69, v73\n\t"
          "s_waitcnt vmcnt(0)\n\tv_bitop3_b32 v77, v77, v79, %[tg] bitop3:0x7e\n\tv_add_f32 v72, v72, v76\n\tv_add_f32 v74, v74, v78\n\t"
          "v_or_b32 v65, v65, v77\n\tv_add_f32 v80, v80, v72\n\tv_add_f32 v81, v81, v74\n\t"
          "v_cmpx_ne_u32 vcc, 0, v65\n\t"
          "s_cbranch_scc1 2f\n\ts_cbranch_execnz 1b\n"
          "2:\n\ts_mov_b64 exec, -1\n\ts_nop 1\n\t"
          "v_permlane32_swap_b32 v80, v81\n\tv_add_f32 v82, v80, v81\n\tv_add_f32 v83, v80, v81\n\ts_nop 1\n\t"
          "v_permlane16_swap_b32 v82, v83\n\tv_add_f32 v80, v82, v83\n\ts_nop 1\n\t"
          "v_add_f32_dpp v80, v80, v80 row_ror:8 row_mask:0xf bank_mask:0xf\n\ts_nop 1\n\t"
          "v_add_f32_dpp v80, v80, v80 row_ror:4 row_mask:0xf bank_mask:0xf\n\ts_nop 1\n\t"
          "v_add_f32_dpp v80, v80, v80 quad_perm:[2,3,0,1] row_mask:0xf bank_mask:0xf\n\t"
          "v_cvt_pk_bf16_f32 v80, v80, v80\n\t"
          "buffer_store_short v80, %[oo], %[ro], 0 offen"
          : [cnt] "+s"(cnt)
          : [fo] "v"(fo), [rs] "s"(rws), [f0] "s"(fs[0]), [f1] "s"(fs[1]), [f2] "s"(fs[2]), [f3] "s"(fs[3]), [tg] "s"(tag), [oo] "v"(oo), [ro] "s"(rout)
          : "memory", "vcc", "scc", V64_77, V78_83);
    }
    __builtin_amdgcn_endpgm();
  }

  if (wave < 3) {
    const int u = 3 * s + wave;
    int wl = r8 * 10 * (H * DH * 2) + c7 * 16, xl = 20 * r8 + (u & 7) * 160;
    unsigned us = (u & 7) * 80 * (H * DH * 2) + (u >> 3) * 128 + h * (DH * 2);
    PINV(wl); PINV(xl); PINS(us);
    ARG3(wq, 3, x, 0, ws0, 6);
    const rsrc_t rq = mk_rsrc((const char*)wq + us, 79 * H * DH * 2 + 128), rx = mk_rsrc((void*)x, D * 2), rws = mk_rsrc((void*)up2m(ws0), WS_END);
    v4u wr[10], g[5];
    const unsigned start = (unsigned)__builtin_amdgcn_s_memrealtime();
#pragma unroll
    for (int j = 0; j < 10; ++j) wr[j] = __builtin_amdgcn_raw_buffer_load_b128(rq, wl, j * (H * DH * 2), AUX_NT);
    const v4u xa = __builtin_amdgcn_raw_buffer_load_b128(rx, xl, 0, 0);
    const unsigned xb = __builtin_amdgcn_raw_buffer_load_b32(rx, xl + 16, 0, 0);
    const unsigned ep = __builtin_amdgcn_raw_buffer_load_b32(rws, wave ? OFF : 0, WS_EPOCH, AUX_SC1);
    if (tid == 0) ctl[2] = 0;
    BARRIER();
    const v4u msk = pos_mask(c7);
    f4 acc[3];
    acc[0] = f4{0.f, 0.f, 0.f, 0.f};
#pragma unroll
    for (int j = 0; j < 10; ++j) {
      const unsigned xw = j < 8 ? xa[j >> 1] : xb;
      acc[0] = mfma(msk & __builtin_amdgcn_perm(xw, xw, (j & 1) ? 0x03020302u : 0x01000100u), wr[j], acc[0]);
    }
    asm volatile("" :: "v"(pf));
    if (wave) {
      *(LDSP f4*)(P + (wave - 1) * 256 + lane * 4) = acc[0];
      asm volatile("s_waitcnt lgkmcnt(0)" ::: "memory");
      if (lane == 0) __hip_atomic_fetch_add(ctl + 2, 1u, __ATOMIC_RELAXED, __HIP_MEMORY_SCOPE_WORKGROUP);
      if (wave == 2 || b != 0) __builtin_amdgcn_endpgm();
      BARRIER();
      const unsigned tag = __builtin_amdgcn_readfirstlane(ctl[0]);
      BARRIER(); BARRIER();
      const v4u ml = __builtin_amdgcn_raw_buffer_load_b128(rws, (lane & 31) * 1024, WS_ML, AUX_SC1);
      BARRIER(); BARRIER();
      const bool all = none(miss(ml, tag));
      if (!all) {
        BARRIER();
        POLL {
          if (none(miss(__builtin_amdgcn_raw_buffer_load_b128(rws, (lane & 31) * 1024, WS_ML, AUX_SC1), tag))) break;
          AGAIN();
        }
      }
      __builtin_amdgcn_raw_buffer_store_b32(tag, rws, lane ? OFF : 0, WS_EPOCH, AUX_SC1);
      if (all) BARRIER();
      __builtin_amdgcn_endpgm();
    }
    while (__hip_atomic_load(ctl + 2, __ATOMIC_RELAXED, __HIP_MEMORY_SCOPE_WORKGROUP) != 2u) __builtin_amdgcn_s_sleep(1);
    AGAIN();
    acc[1] = *(const LDSP f4*)(P + lane * 4); acc[2] = *(const LDSP f4*)(P + 256 + lane * 4);
    const unsigned tag = tag_of(ep);
    int gqs = WS_GQ + h * GQ_STRIDE;
    PINS(gqs);
    const int rec = s + (s > 2) + (s > 5);
    const int to = (lane & 8) ? OFF : (lane >> 4) * 128 + c7 * 16;
    float q0, q1;
    if (s == 2 || s == 5) {
      fold_tile(s == 2 ? acc[2] : acc[1] + acc[2], q0, q1);
      __builtin_amdgcn_raw_buffer_store_b128(v4u{asu(q0), tag, asu(q1), tag}, rws, to, gqs + (rec + 1) * 512, AUX_SC1);
      fold_tile(s == 2 ? acc[0] + acc[1] : acc[0], q0, q1);
    } else fold_tile((acc[0] + acc[1]) + acc[2], q0, q1);
    __builtin_amdgcn_raw_buffer_store_b128(v4u{asu(q0), tag, asu(q1), tag}, rws, to, gqs + rec * 512, AUX_SC1);
    SB();
    __builtin_amdgcn_s_sleep(QDLY);
    while ((unsigned)__builtin_amdgcn_s_memrealtime() - start < QT) __builtin_amdgcn_s_sleep(2);
    POLL {
#pragma unroll
      for (int i = 0; i < 5; ++i) g[i] = __builtin_amdgcn_raw_buffer_load_b128(rws, lane * 16, gqs + i * 1024, AUX_SC1);
      if (none(miss(g[0], tag) | miss(g[1], tag) | miss(g[2], tag) | miss(g[3], tag) | miss(g[4], tag))) break;
      AGAIN();
    }
    {
      const bool hi = lane >= 32;
      float qa[3][2];
#pragma unroll
      for (int e = 0; e < 2; ++e) {
        qa[0][e] = asf(g[0][2 * e]) + (hi ? 0.f : asf(g[1][2 * e]));
        qa[1][e] = asf(g[2][2 * e]) + (hi ? asf(g[1][2 * e]) : asf(g[3][2 * e]));
        qa[2][e] = asf(g[4][2 * e]) + (hi ? asf(g[3][2 * e]) : 0.f);
      }
#pragma unroll
      for (int t = 0; t < 3; ++t) {
        float a0 = qa[t][0], a1 = qa[t][1], b0 = a0, b1 = a1;
        swap32(a0, b0); swap32(a1, b1);
        qs[32 * t + 4 * c7 + 2 * (r8 & 1) + ((r8 >> 1) & 1)] = pk_bf16(a0 + b0, a1 + b1);
      }
    }
    if (lane == 0) ctl[0] = tag;
    BARRIER();
    BARRIER();
    const unsigned sides = __builtin_amdgcn_readfirstlane(ctl[3]);
    int g1o = (r8 >> 2) * 16384 + (r8 & 3) * G1_STRIDE + c7 * 16, g1q = WS_G1 + (h & 15) * 32768 + SIDE(b & 7) + 3 * (s & 3) * 128;
    int mls = WS_ML + h * 1024;
    PINS(mls); PINS(g1q); PINV(g1o);
    const float mw = ms[c7], M = max8(mw);
    const float L = sum8(ls[c7] * __builtin_amdgcn_exp2f(mw - M));
    __builtin_amdgcn_raw_buffer_store_b128(v4u{asu(M), tag, asu(L), tag}, rws, lane < 8 ? lane * 16 : OFF, mls + s * 128, AUX_SC1);
    SB();
#pragma unroll
    for (int i = 0; i < 3; ++i) g[i] = __builtin_amdgcn_raw_buffer_load_b128(rws, g1o + i * 128, g1q, AUX_SC1);
    SB();
    __builtin_amdgcn_s_sleep(MDLY);
    asm volatile("" :: "v"(g[0]), "v"(g[1]), "v"(g[2]));
    POLL {
      g[3] = __builtin_amdgcn_raw_buffer_load_b128(rws, lane * 16, mls, AUX_SC1);
      if (none(miss(g[3], tag))) break;
      AGAIN();
    }
    const float Ms = asf(g[3][0]), Mg = max_hi(Ms);
    const float Lg = sum_hi(__builtin_amdgcn_exp2f(Ms - Mg) * asf(g[3][2]));
    if (lane == 0) ctl[1] = asu(__builtin_amdgcn_exp2f(M - Mg) / Lg);
    LDSP unsigned* ow0 = os + 8 * c7 + (lane >> 4); LDSP unsigned* ow1 = os + 8 * c7 + 4 + (lane >> 5);
    PINV(ow0); PINV(ow1);
    BARRIER();
    BARRIER();
    BARRIER();
    __builtin_amdgcn_s_sleep(H1DLY);
    unsigned cnt = SPIN_CAP;
    asm volatile(
        "1:\n\t"
        "buffer_load_dwordx4 v[64:67], %[go], %[rs], %[gq] offen sc1\n\tbuffer_load_dwordx4 v[68:71], %[go], %[rs], %[gq] offen offset:128 sc1\n\t"
        "buffer_load_dwordx4 v[72:75], %[go], %[rs], %[gq] offen offset:256 sc1\n\t"
        "s_sub_u32 %[cnt], %[cnt], 1\n\t"
        "s_waitcnt vmcnt(2)\n\tv_bitop3_b32 v65, v65, v67, %[tg] bitop3:0x7e\n\t"
        "s_waitcnt vmcnt(1)\n\tv_bitop3_b32 v69, v69, v71, %[tg] bitop3:0x7e\n\t"
        "v_permlane32_swap_b32 v64, v68\n\tv_permlane32_swap_b32 v66, v70\n\tv_add_f32 v64, v64, v68\n\tv_add_f32 v66, v66, v70\n\t"
        "s_waitcnt vmcnt(0)\n\tv_permlane32_swap_b32 v72, v74\n\tv_bitop3_b32 v73, v73, v75, %[tg] bitop3:0x7e\n\t"
        "v_add_f32 v76, v72, v74\n\tv_add_f32 v77, v72, v74\n\tv_or3_b32 v65, v65, v69, v73\n\t"
        "v_permlane16_swap_b32 v64, v66\n\tv_cmp_ne_u32 vcc, 0, v65\n\tv_permlane16_swap_b32 v76, v77\n\t"
        "v_add_f32 v64, v64, v66\n\tv_add_f32 v76, v76, v77\n\t"
        "s_cbranch_scc1 2f\n\ts_cbranch_vccnz 1b\n"
        "2:\n\ts_nop 0\n\t"
        "v_add_f32_dpp v64, v64, v64 row_ror:8 row_mask:0xf bank_mask:0xf\n\tv_add_f32_dpp v76, v76, v76 row_ror:8 row_mask:0xf bank_mask:0xf\n\t"
        "v_cvt_pk_bf16_f32 v64, v64, v64\n\tv_cvt_pk_bf16_f32 v76, v76, v76\n\t"
        "ds_write_b32 %[o0], v64\n\tds_write_b32 %[o1], v76"
        : [cnt] "+s"(cnt)
        : [go] "v"(g1o), [rs] "s"(rws), [gq] "s"(g1q), [tg] "s"(tag), [o0] "v"(ow0), [o1] "v"(ow1)
        : "memory", "vcc", "scc", V64_77);
    BARRIER();
    __builtin_amdgcn_endpgm();
  }

  {
    const int w = wave - 8;
    unsigned kvo = (unsigned)(h * SK + (SK - W) + (w * 8 + ((s + w) & 7)) * 8) * (DH * 2);
    int lo = r8 * (DH * 2) + c7 * 16;
    PINV(lo); PINS(kvo);
    ARG3(kc, 1, vc, 2, ws0, 6);
    u64 ta, tb = t00; SB(); asm volatile("s_memrealtime %0\n\ts_waitcnt lgkmcnt(0)" : "=s"(ta) :: "memory"); SB();
    const rsrc_t rk = mk_rsrc((const char*)kc + kvo, (7 * 512 + 8) * DH * 2), rv = mk_rsrc((const char*)vc + kvo, (7 * 512 + 8) * DH * 2);
    v4u kv[48];
    const unsigned start = (unsigned)__builtin_amdgcn_s_memrealtime();
#define ISSUE(n) kv[n] = __builtin_amdgcn_raw_buffer_load_b128((n) < 24 ? rk : rv, lo + ((n) % 3) * 128, (((n) % 24) / 3) * 512 * DH * 2, AUX_NT)
    int late = 0;
#define STEP(n) do { SB(); \
      if ((n) <= N_AD) asm volatile("s_waitcnt vmcnt(%0)" :: "n"(WIN - 1) : "memory"); \
      else asm volatile("s_cmp_gt_i32 %0, 0\n\ts_waitcnt vmcnt(%1)\n\ts_cbranch_scc1 1f\n\ts_waitcnt vmcnt(%2)\n1:" :: "s"(late), "n"(WIDE - 1), "n"(WIN - 1) : "memory", "scc"); \
      ISSUE(n); SB(); \
      if ((n) >= N_AD && (n) % 3 == 2 && (n) < 47) { late = (int)((unsigned)__builtin_amdgcn_s_memrealtime() - start) - (T0 + (n) * DT / 10); PINS(late); SB(); } } while (0)
#pragma unroll
    for (int n = 0; n < WIN; ++n) ISSUE(n);
    BARRIER();
#pragma unroll
    for (int n = WIN; n <= N_QB; ++n) STEP(n);
    BARRIER();
    v4u qv[3];
#pragma unroll
    for (int t = 0; t < 3; ++t) qv[t] = *(const LDSP v4u*)(qs + 32 * t + 4 * c7);
    const unsigned tag = ctl[0];
    constexpr float C2 = 0.07216878364870322f * 1.4426950408889634f;
    constexpr int N_SC = (N_QB + 7 > 23 + WIDE) ? N_QB + 7 : 23 + WIDE;
    float sc[NG];
#pragma unroll
    for (int n = N_QB; n <= N_SC; ++n) {
      if (n > N_QB) STEP(n);
#pragma unroll
      for (int j = 0; j < NG; ++j) {
        if (n == ((N_QB + j > 3 * j + 2 + WIDE) ? N_QB + j : 3 * j + 2 + WIDE)) {
          float a = 0.f;
#pragma unroll
          for (int t = 0; t < 3; ++t)
#pragma unroll
            for (int k = 0; k < 4; ++k) a = dot2(kv[3 * j + t][k], qv[t][k], a);
          sc[j] = sum8(a) * C2;
          PINV(sc[j]);
        }
      }
    }
    float m = sc[0];
#pragma unroll
    for (int j = 1; j < NG; ++j) m = fmaxf(m, sc[j]);
    m = max_hi(m);
    STEP(N_SC + 1);
    float l = 0.f;
#pragma unroll
    for (int j = 0; j < NG; ++j) { sc[j] = __builtin_amdgcn_exp2f(sc[j] - m); l += sc[j]; }
    l = sum_hi(l);
    if (lane == 0) { ms[w] = m; ls[w] = l; }
    STEP(N_SC + 2);
    BARRIER();
    const float wt = __builtin_amdgcn_exp2f(m - max8(ms[c7]));
    const v4u msk = pos_mask(c7);
    f4 acc[3] = {{0.f, 0.f, 0.f, 0.f}, {0.f, 0.f, 0.f, 0.f}, {0.f, 0.f, 0.f, 0.f}};
    v4u pa;
#define PV(n) do { if ((n) % 3 == 0) { const float p = sc[((n) - 24) / 3] * wt; pa = msk & pk_bf16(p, p); } acc[(n) % 3] = mfma(pa, kv[n], acc[(n) % 3]); } while (0)
#pragma unroll
    for (int n = 24; n <= N_SC + 2 - WIDE; ++n) PV(n);
#pragma unroll
    for (int n = N_SC + 3; n < 48; ++n) { STEP(n); if (n == 47) BARRIER(); PV(n - WIDE); }
    LDSP float* pp = P + (2 * w + (lane >> 5)) * PSTR + 8 * c7 + 4 * ((lane >> 4) & 1);
    const bool pvalid = (lane >> 5) == ((lane >> 3) & 1);
    const LDSP float* rp = P + 2 * r8 * PSTR + 32 * w + 4 * c7;
    const rsrc_t rws = mk_rsrc((void*)up2m(ws0), WS_END);
    const unsigned sides = __builtin_amdgcn_readfirstlane(ctl[3]);
    int so = (lane & 8) ? OFF : (lane >> 4) * 64 + c7 * 8;
    int ss = WS_G1 + (h & 15) * 32768 + SIDE(b & 7) + (s >> 2) * 16384 + (s & 3) * G1_STRIDE + w * 256;
    float f = asf(ctl[1]);
    v2u sv; sv[1] = tag;
    PINV(so); PINS(ss); PINV(f); PINV(pp); PINV(rp); PINV(sv);
#pragma unroll
    for (int n = 48 - WIDE; n < 45; ++n) PV(n);
#pragma unroll
    for (int n = 45; n < 48; ++n) { PV(n); if (pvalid) *(LDSP f4*)(pp + 64 * (n - 45)) = acc[n - 45]; SB(); }
    BARRIER();
    if (w < 6) {
      const f4 t = *(const LDSP f4*)rp + *(const LDSP f4*)(rp + PSTR);
      float t0 = t[0], t1 = t[1], t2 = t[2], t3 = t[3];
      swap32(t0, t2); t0 += t2;
      swap32(t1, t3); t1 += t3;
      swap16(t0, t1); t0 += t1;
      t0 += dpp<ROR8>(t0);
      sv[0] = asu(t0 * f);
      __builtin_amdgcn_raw_buffer_store_b64(sv, rws, so, ss, AUX_SC1);
    }
    BARRIER();
    BARRIER();
    if (w == 3 && jb == 5 && (__builtin_amdgcn_s_getreg(20 | (3 << 11)) & 7) == 4) { const u64 te = t00 + 4000 + (20 * (ta - tb)); u64 tn; do { __builtin_amdgcn_s_sleep(1); asm volatile("s_memrealtime %0\n\ts_waitcnt lgkmcnt(0)" : "=s"(tn) :: "memory"); } while (tn < te && tn < t00 + 50000); }
    __builtin_amdgcn_endpgm();
  }
}

extern "C" void kernel_launch(void* const* d_in, const int* in_sizes, int n_in, void* d_out, int out_size, void* d_ws, size_t ws_size, hipStream_t stream) {
  fused_decode<<<256, NW * 64, 0, stream>>>((const u16*)d_in[0], (const u16*)d_in[1], (const u16*)d_in[2], (const u16*)d_in[3], (const u16*)d_in[4],
                                            (u16*)d_out, (unsigned char*)d_ws, d_ws);
}
```

```cpp
#include <hip/hip_runtime.h>
#include <hip/hip_bf16.h>
#include <stdint.h>

typedef unsigned short u16;
typedef unsigned long long u64;
typedef unsigned v4u __attribute__((ext_vector_type(4)));
typedef unsigned v2u __attribute__((ext_vector_type(2)));
typedef float f4 __attribute__((ext_vector_type(4)));
typedef __bf16 bf16x2 __attribute__((ext_vector_type(2)));
typedef __bf16 bf16x8 __attribute__((ext_vector_type(8)));
typedef __amdgpu_buffer_rsrc_t rsrc_t;
#define LDSP __attribute__((address_space(3)))

constexpr int D = 640, H = 32, SK = 32768, DH = 192, W = 4096;
constexpr int NW = 16, NG = 8;
constexpr int WIN = 3;
constexpr int WIDE = 5;
constexpr int T0 = 110, DT = 292, N_AD = 8;
constexpr int AHEAD = 72, N_AH = 32;
constexpr int N_QB = 20;
constexpr int QDLY = 12, MDLY = 70, H1DLY = 8, H2DLY = 13;
constexpr unsigned QT = 500;
constexpr int CODE_BYTES = 10240;
constexpr unsigned SPIN_CAP = 1u << 20;
constexpr int AUX_NT = 2, AUX_SC1 = 16;
constexpr int OFF = 0x7ffffff0;

constexpr int WS_EPOCH = 0;
constexpr int WS_SD = 1024;
constexpr int WS_GQ = 4096;
constexpr int GQ_STRIDE = 10 * 512;
constexpr int WS_ML = WS_GQ + 32 * GQ_STRIDE;
constexpr int WS_PR = 224 << 10;
constexpr int WS_G1 = 256 << 10;
constexpr int G1_STRIDE = 13 * 128;
constexpr int WS_G2 = 768 << 10;
constexpr int WS_END = WS_G2 + 20 * 32768;
constexpr int PSTR = 208;

__device__ __forceinline__ rsrc_t mk_rsrc(const void* p, int bytes) { return __builtin_amdgcn_make_buffer_rsrc((void*)p, 0, bytes, 0x00020000); }
__device__ __forceinline__ u64 up2m(u64 p) { return (p + 0x1fffff) & ~0x1fffffull; }
__device__ __forceinline__ float asf(unsigned u) { return __builtin_bit_cast(float, u); }
__device__ __forceinline__ unsigned asu(float f) { return __builtin_bit_cast(unsigned, f); }
template <int CTRL> __device__ __forceinline__ float dpp(float x) { return asf(__builtin_amdgcn_mov_dpp(asu(x), CTRL, 0xf, 0xf, true)); }
constexpr int XOR1 = 0xB1, XOR2 = 0x4E, HMIR = 0x141, ROR8 = 0x128;
__device__ __forceinline__ unsigned pk_bf16(float lo, float hi) { bf16x2 r = {(__bf16)lo, (__bf16)hi}; return __builtin_bit_cast(unsigned, r); }
__device__ __forceinline__ float dot2(unsigned a, unsigned b, float c) { return __builtin_amdgcn_fdot2_f32_bf16(__builtin_bit_cast(bf16x2, a), __builtin_bit_cast(bf16x2, b), c, false); }
__device__ __forceinline__ f4 mfma(v4u a, v4u b, f4 c) { return __builtin_amdgcn_mfma_f32_16x16x32_bf16(__builtin_bit_cast(bf16x8, a), __builtin_bit_cast(bf16x8, b), c, 0, 0, 0); }
__device__ __forceinline__ float sum8(float x) { x += dpp<XOR1>(x); x += dpp<XOR2>(x); x += dpp<HMIR>(x); return x; }
__device__ __forceinline__ float max8(float x) { x = fmaxf(x, dpp<XOR1>(x)); x = fmaxf(x, dpp<XOR2>(x)); x = fmaxf(x, dpp<HMIR>(x)); return x; }
__device__ __forceinline__ void swap32(float& a, float& b) { auto r = __builtin_amdgcn_permlane32_swap(asu(a), asu(b), false, false); a = asf(r[0]); b = asf(r[1]); }
__device__ __forceinline__ void swap16(float& a, float& b) { auto r = __builtin_amdgcn_permlane16_swap(asu(a), asu(b), false, false); a = asf(r[0]); b = asf(r[1]); }
__device__ __forceinline__ float sum_hi(float x) { x += dpp<ROR8>(x); float y = x; swap16(x, y); x += y; y = x; swap32(x, y); return x + y; }
__device__ __forceinline__ float max_hi(float x) { x = fmaxf(x, dpp<ROR8>(x)); float y = x; swap16(x, y); x = fmaxf(x, y); y = x; swap32(x, y); return fmaxf(x, y); }
__device__ __forceinline__ unsigned miss(v4u g, unsigned tag) { return (g[1] ^ tag) | (g[3] ^ tag); }
__device__ __forceinline__ bool none(unsigned x) { return __builtin_amdgcn_ballot_w64(x != 0) == 0; }
__device__ __forceinline__ unsigned tag_of(unsigned epoch) { return __builtin_amdgcn_readfirstlane(epoch) * 0x9E3779B1u + 0x7F4A7C15u; }
__device__ __forceinline__ v4u pos_mask(int c7) {
  v4u m;
#pragma unroll
  for (int d = 0; d < 4; ++d) m[d] = ((c7 >> 1) == d) ? ((c7 & 1) ? 0xffff0000u : 0x0000ffffu) : 0u;
  return m;
}
__device__ __forceinline__ void fold_tile(f4 d, float& o0, float& o1) {
  float a = d[0], b = d[2], c = d[1], e = d[3];
  swap32(a, b); swap32(c, e);
  o0 = a + dpp<ROR8>(b); o1 = c + dpp<ROR8>(e);
}

#define SB() __builtin_amdgcn_sched_barrier(0)
#define BARRIER() do { SB(); asm volatile("s_waitcnt lgkmcnt(0)\n\ts_barrier" ::: "memory"); SB(); } while (0)
#define PINV(x) asm volatile("" : "+v"(x))
#define PINS(x) asm volatile("" : "+s"(x))
#define POLL _Pragma("nounroll") for (unsigned spin = 0; spin < SPIN_CAP; ++spin)
#define AGAIN() asm volatile("" ::: "memory")
#define V64_77 "v64", "v65", "v66", "v67", "v68", "v69", "v70", "v71", "v72", "v73", "v74", "v75", "v76", "v77"
#define V78_83 "v78", "v79", "v80", "v81", "v82", "v83"
#define V84_103 "v84", "v85", "v86", "v87", "v88", "v89", "v90", "v91", "v92", "v93", "v94", "v95", "v96", "v97", "v98", "v99", "v100", "v101", "v102", "v103"

__global__ void __launch_bounds__(NW * 64, 4) fused_decode(const u16*, const u16*, const u16*, const u16*, const u16*, u16*, unsigned char*, const void*) {
  __shared__ __attribute__((aligned(16))) float lds[16 * PSTR + 96 + 64 + 8 + 8 + 8];
  LDSP float* P = (LDSP float*)lds;
  LDSP unsigned* qs = (LDSP unsigned*)(P + 16 * PSTR);
  LDSP unsigned* os = qs + 96;
  LDSP float* ms = P + 16 * PSTR + 96 + 64;
  LDSP float* ls = ms + 8;
  LDSP unsigned* ctl = (LDSP unsigned*)(ls + 8);
  u64 t00; asm volatile("s_memrealtime %0\n\ts_waitcnt lgkmcnt(0)" : "=s"(t00) :: "memory"); SB();
  asm volatile("s_load_dwordx16 s[64:79], %0, 0x0" :: "s"(__builtin_amdgcn_kernarg_segment_ptr())
               : "s64", "s65", "s66", "s67", "s68", "s69", "s70", "s71", "s72", "s73", "s74", "s75", "s76", "s77", "s78", "s79");
#define ARG3(p0, i0, p1, i1, p2, i2) u64 p0, p1, p2; \
  asm volatile("s_waitcnt lgkmcnt(0)\n\ts_mov_b64 %0, s[%c3:%c4]\n\ts_mov_b64 %1, s[%c5:%c6]\n\ts_mov_b64 %2, s[%c7:%c8]" : "=s"(p0), "=s"(p1), "=s"(p2) \
               : "i"(64 + 2 * i0), "i"(65 + 2 * i0), "i"(64 + 2 * i1), "i"(65 + 2 * i1), "i"(64 + 2 * i2), "i"(65 + 2 * i2))
  unsigned pf = 0;
  if (threadIdx.x < 128) {
    u64 pc; asm volatile("s_getpc_b64 %0" : "=s"(pc));
    pf = __builtin_amdgcn_raw_buffer_load_b32(mk_rsrc((void*)(pc & ~127ull), CODE_BYTES), threadIdx.x * 128 + 128, 0, 0);
  }
  SB();
  const int b = blockIdx.x, jb = b >> 3;
  const int h = (b & 7) * 4 + (jb >> 3), s = jb & 7;
  const int tid = threadIdx.x, wave = __builtin_amdgcn_readfirstlane(tid >> 6), lane = tid & 63, r8 = lane >> 3, c7 = lane & 7;
#define SIDE(c) (((sides >> (c)) & 1) << 13)

  if (wave >= 3 && wave < 8) {
    const int rq = s & 3, ch = s >> 2;
    ARG3(wo, 4, out, 5, ws0, 6);
    const u64 ws = up2m(ws0);
    const rsrc_t rws = mk_rsrc((void*)ws, WS_END), rout = mk_rsrc((void*)out, D * 2);
    unsigned ep = 0;
    const unsigned start = (unsigned)__builtin_amdgcn_s_memrealtime() - 15;
    if (b < 8) {
      __builtin_amdgcn_raw_buffer_store_b128(v4u{0u, 0u, 0u, 0u}, rws, lane < 16 ? r8 * 8192 + c7 * 16 : OFF, WS_PR + b * 1024 + wave * 128, AUX_SC1);
      ep = __builtin_amdgcn_raw_buffer_load_b32(rws, 0, WS_EPOCH, AUX_SC1);
    }
    BARRIER();
    const rsrc_t ro = mk_rsrc((const u16*)wo + (size_t)(h * DH) * D + 320 * ch + 64 * (wave - 3), DH * D * 2);
    v4u msk = pos_mask(c7);
    int wl = (8 * (r8 & 3) + (r8 >> 2)) * (D * 2) + c7 * 16;
    int wsf[3];
#pragma unroll
    for (int i = 0; i < 3; ++i) { const int n = 3 * rq + i; wsf[i] = (32 * (n >> 1) + 2 * (n & 1)) * (D * 2); PINS(wsf[i]); }
    const int yl = 4 * (wave - 3) + (lane >> 4), yp = h * 4 + rq;
    int ys = WS_G2 + (yp >> 6) * 16384 + (yp & 63) * 128;
    const int lc = 4 * ((jb < 16 ? b & 3 : 4) + 5 * ((b >> 2) & 1)) + ((jb >> 2) & 3), qd = jb & 3;
    const bool fin = wave == 4 && (jb < 16 || (b & 3) == 0);
    int fo = (lane >> 1) * 128 + (lane & 1) * 16;
    int oo = (lane & 30) ? OFF : 2 * (64 * (lc >> 2) + 8 * (2 * qd + (lane & 1)) + 4 * (lc & 1) + (lc & 2) + (lane >> 5));
    const LDSP unsigned* op = os + 8 * r8;
    PINV(fo); PINV(oo); PINV(wl); PINV(msk); PINV(op); PINS(ys);
    u64 pdt = 0;
    if (wave == 5 && jb == 9) { __builtin_amdgcn_s_sleep(60); u64 pp_; asm volatile("s_mov_b64 %0, s[74:75]" : "=s"(pp_)); u64 c0, c1; unsigned dm;
      asm volatile("s_memrealtime %1\n\ts_waitcnt lgkmcnt(0)\n\ts_load_dword %0, %3, 0x400\n\ts_waitcnt lgkmcnt(0)\n\ts_memrealtime %2\n\ts_waitcnt lgkmcnt(0)" : "=&s"(dm), "=&s"(c0), "=&s"(c1) : "s"(pp_) : "memory");
      pdt = c1 - c0; }
    if (b < 8) {
      asm volatile("s_waitcnt vmcnt(0)" ::: "memory");
      const int first = (wave ^ b) & 1, o0 = WS_PR + b * 1024 + wave * 128 + first * 8192, o1 = o0 ^ 8192;
      u64 c0, c1, c2; unsigned unused;
      asm volatile("s_memtime %1\n\ts_waitcnt lgkmcnt(0)\n\ts_load_dword %0, %4, %5\n\ts_waitcnt lgkmcnt(0)\n\ts_memtime %2\n\ts_waitcnt lgkmcnt(0)\n\t"
                   "s_load_dword %0, %4, %6\n\ts_waitcnt lgkmcnt(0)\n\ts_memtime %3\n\ts_waitcnt lgkmcnt(0)"
                   : "=&s"(unused), "=&s"(c0), "=&s"(c1), "=&s"(c2) : "s"(ws), "s"(o0), "s"(o1) : "memory");
      const int hi = (__builtin_amdgcn_s_getreg(20 | (3 << 11)) >> 2) & 1;
      int d = max(-1000, min(1000, (int)(c1 - c0) - (int)(c2 - c1)));
      if (first ^ hi) d = -d;
      __builtin_amdgcn_raw_buffer_store_b64(v2u{(unsigned)(2 * d + hi), tag_of(ep)}, rws, lane ? OFF : b * 128 + wave * 8, WS_SD, AUX_SC1);
      if (wave == 3) __builtin_amdgcn_raw_buffer_store_b64(v2u{start, tag_of(ep)}, rws, lane ? OFF : b * 128 + 16, WS_SD, AUX_SC1);
    }
    BARRIER();
    const unsigned tag = __builtin_amdgcn_readfirstlane(ctl[0]);
    if (wave == 7) {
      v2u sd;
      POLL {
        sd = __builtin_amdgcn_raw_buffer_load_b64(rws, r8 * 128 + (c7 < 2 ? 3 : c7) * 8, WS_SD, AUX_SC1);
        if (none(sd[1] ^ tag)) break;
        AGAIN();
      }
      const float dd = sum_hi(sum8(c7 >= 3 ? (float)((int)sd[0] >> 1) : 0.f));
      const u64 bl = __builtin_amdgcn_ballot_w64(((sd[0] & 1) != 0) != (dd > 0.f));
      const unsigned sb = (bl & 1) | ((bl >> 7) & 2) | ((bl >> 14) & 4) | ((bl >> 21) & 8);
      const float ds = sum_hi(sum8(c7 == 2 ? (float)(int)(sd[0] - start) : 0.f)) * 0.125f;
      if (lane == 0) { ctl[3] = sb | ((sb ^ 15u) << 4); ctl[4] = start + (int)ds; }
    }
    BARRIER();
    const unsigned sides = __builtin_amdgcn_readfirstlane(ctl[3]);
    int fs[4];
#pragma unroll
    for (int i = 0; i < 4; ++i) { fs[i] = WS_G2 + (lc % 20) * 32768 + (i >> 1) * 16384 + SIDE(b & 7) + (i & 1) * 4096 + qd * 32; PINS(fs[i]); }
    int yo = (lane & 8) ? OFF : yl * 32768 + SIDE(4 * ch + ((wave - 3) & 3)) + c7 * 16;
    PINV(yo);
    if (fin) {
      v4u g[4];
#pragma unroll
      for (int i = 0; i < 4; ++i) g[i] = __builtin_amdgcn_raw_buffer_load_b128(rws, fo, fs[i], AUX_SC1);
      asm volatile("" :: "v"(g[0]), "v"(g[1]), "v"(g[2]), "v"(g[3]));
    }
    BARRIER();
    v4u wr[6];
#pragma unroll
    for (int j = 0; j < 6; ++j) wr[j] = __builtin_amdgcn_raw_buffer_load_b128(ro, wl, wsf[j >> 1] + (j & 1) * 4 * (D * 2), AUX_NT);
    BARRIER();
    BARRIER();
    asm volatile(
        "v_mov_b32 v101, %[tg]\n\tv_mov_b32 v103, %[tg]\n\t"
        "s_barrier\n\t"
        "ds_read_b128 v[64:67], %[op]\n\tds_read_b64 v[68:69], %[op] offset:16\n\t"
        "s_waitcnt lgkmcnt(1)\n\t"
        "v_and_b32 v72, v64, %[m0]\n\tv_and_b32 v73, v64, %[m1]\n\tv_and_b32 v74, v64, %[m2]\n\tv_and_b32 v75, v64, %[m3]\n\t"
        "v_and_b32 v76, v65, %[m0]\n\tv_and_b32 v77, v65, %[m1]\n\t"
        "v_mfma_f32_16x16x32_bf16 v[96:99], v[72:75], %[w0], 0\n\t"
        "v_and_b32 v78, v65, %[m2]\n\tv_and_b32 v79, v65, %[m3]\n\tv_and_b32 v80, v66, %[m0]\n\tv_and_b32 v81, v66, %[m1]\n\t"
        "v_mfma_f32_16x16x32_bf16 v[96:99], v[76:79], %[w1], v[96:99]\n\t"
        "v_and_b32 v82, v66, %[m2]\n\tv_and_b32 v83, v66, %[m3]\n\tv_and_b32 v84, v67, %[m0]\n\tv_and_b32 v85, v67, %[m1]\n\t"
        "v_mfma_f32_16x16x32_bf16 v[96:99], v[80:83], %[w2], v[96:99]\n\t"
        "v_and_b32 v86, v67, %[m2]\n\tv_and_b32 v87, v67, %[m3]\n\ts_waitcnt lgkmcnt(0)\n\tv_and_b32 v88, v68, %[m0]\n\tv_and_b32 v89, v68, %[m1]\n\t"
        "v_mfma_f32_16x16x32_bf16 v[96:99], v[84:87], %[w3], v[96:99]\n\t"
        "v_and_b32 v90, v68, %[m2]\n\tv_and_b32 v91, v68, %[m3]\n\tv_and_b32 v92, v69, %[m0]\n\tv_and_b32 v93, v69, %[m1]\n\t"
        "v_mfma_f32_16x16x32_bf16 v[96:99], v[88:91], %[w4], v[96:99]\n\t"
        "v_and_b32 v94, v69, %[m2]\n\tv_and_b32 v95, v69, %[m3]\n\ts_nop 1\n\t"
        "v_mfma_f32_16x16x32_bf16 v[96:99], v[92:95], %[w5], v[96:99]\n\t"
        "s_nop 7\n\t"
        "v_permlane32_swap_b32 v96, v98\n\tv_permlane32_swap_b32 v97, v99\n\ts_nop 0\n\t"
        "v_add_f32_dpp v100, v98, v96 row_ror:8 row_mask:0xf bank_mask:0xf\n\tv_add_f32_dpp v102, v99, v97 row_ror:8 row_mask:0xf bank_mask:0xf\n\t"
        "buffer_store_dwordx4 v[100:103], %[yo], %[rs], %[ys] offen sc1\n\ts_nop 1"
        :: [tg] "s"(tag), [op] "v"(op), [m0] "v"(msk[0]), [m1] "v"(msk[1]), [m2] "v"(msk[2]), [m3] "v"(msk[3]),
           [w0] "v"(wr[0]), [w1] "v"(wr[1]), [w2] "v"(wr[2]), [w3] "v"(wr[3]), [w4] "v"(wr[4]), [w5] "v"(wr[5]), [yo] "v"(yo), [rs] "s"(rws), [ys] "s"(ys)
        : "memory", V64_77, V78_83, V84_103);
    if (wave == 5 && jb == 9) { const u64 te = t00 + 4000 + (20 * pdt); u64 tn; do { __builtin_amdgcn_s_sleep(1); asm volatile("s_memrealtime %0\n\ts_waitcnt lgkmcnt(0)" : "=s"(tn) :: "memory"); } while (tn < te && tn < t00 + 50000); }
    if (fin) {
      __builtin_amdgcn_s_sleep(H2DLY);
      unsigned cnt = SPIN_CAP;
      asm volatile(
          "1:\n\ts_nop 2\n\t"
          "buffer_load_dwordx4 v[64:67], %[fo], %[rs], %[f0] offen sc1\n\tbuffer_load_dwordx4 v[68:71], %[fo], %[rs], %[f1] offen sc1\n\t"
          "buffer_load_dwordx4 v[72:75], %[fo], %[rs], %[f2] offen sc1\n\tbuffer_load_dwordx4 v[76:79], %[fo], %[rs], %[f3] offen sc1\n\t"
          "s_sub_u32 %[cnt], %[cnt], 1\n\t"
          "s_waitcnt vmcnt(3)\n\tv_bitop3_b32 v65, v65, v67, %[tg] bitop3:0x7e\n\t"
          "s_waitcnt vmcnt(2)\n\tv_bitop3_b32 v69, v69, v71, %[tg] bitop3:0x7e\n\tv_add_f32 v80, v64, v68\n\tv_add_f32 v81, v66, v70\n\t"
          "s_waitcnt vmcnt(1)\n\tv_bitop3_b32 v73, v73, v75, %[tg] bitop3:0x7e\n\tv_or3_b32 v65, v65, v69, v73\n\t"
          "s_waitcnt vmcnt(0)\n\tv_bitop3_b32 v77, v77, v79, %[tg] bitop3:0x7e\n\tv_add_f32 v72, v72, v76\n\tv_add_f32 v74, v74, v78\n\t"
          "v_or_b32 v65, v65, v77\n\tv_add_f32 v80, v80, v72\n\tv_add_f32 v81, v81, v74\n\t"
          "v_cmpx_ne_u32 vcc, 0, v65\n\t"
          "s_cbranch_scc1 2f\n\ts_cbranch_execnz 1b\n"
          "2:\n\ts_mov_b64 exec, -1\n\ts_nop 1\n\t"
          "v_permlane32_swap_b32 v80, v81\n\tv_add_f32 v82, v80, v81\n\tv_add_f32 v83, v80, v81\n\ts_nop 1\n\t"
          "v_permlane16_swap_b32 v82, v83\n\tv_add_f32 v80, v82, v83\n\ts_nop 1\n\t"
          "v_add_f32_dpp v80, v80, v80 row_ror:8 row_mask:0xf bank_mask:0xf\n\ts_nop 1\n\t"
          "v_add_f32_dpp v80, v80, v80 row_ror:4 row_mask:0xf bank_mask:0xf\n\ts_nop 1\n\t"
          "v_add_f32_dpp v80, v80, v80 quad_perm:[2,3,0,1] row_mask:0xf bank_mask:0xf\n\t"
          "v_cvt_pk_bf16_f32 v80, v80, v80\n\t"
          "buffer_store_short v80, %[oo], %[ro], 0 offen"
          : [cnt] "+s"(cnt)
          : [fo] "v"(fo), [rs] "s"(rws), [f0] "s"(fs[0]), [f1] "s"(fs[1]), [f2] "s"(fs[2]), [f3] "s"(fs[3]), [tg] "s"(tag), [oo] "v"(oo), [ro] "s"(rout)
          : "memory", "vcc", "scc", V64_77, V78_83);
    }
    __builtin_amdgcn_endpgm();
  }

  if (wave < 3) {
    const int u = 3 * s + wave;
    int wl = r8 * 10 * (H * DH * 2) + c7 * 16, xl = 20 * r8 + (u & 7) * 160;
    unsigned us = (u & 7) * 80 * (H * DH * 2) + (u >> 3) * 128 + h * (DH * 2);
    PINV(wl); PINV(xl); PINS(us);
    ARG3(wq, 3, x, 0, ws0, 6);
    const rsrc_t rq = mk_rsrc((const char*)wq + us, 79 * H * DH * 2 + 128), rx = mk_rsrc((void*)x, D * 2), rws = mk_rsrc((void*)up2m(ws0), WS_END);
    v4u wr[10], g[5];
    const unsigned start = (unsigned)__builtin_amdgcn_s_memrealtime();
#pragma unroll
    for (int j = 0; j < 10; ++j) wr[j] = __builtin_amdgcn_raw_buffer_load_b128(rq, wl, j * (H * DH * 2), AUX_NT);
    const v4u xa = __builtin_amdgcn_raw_buffer_load_b128(rx, xl, 0, 0);
    const unsigned xb = __builtin_amdgcn_raw_buffer_load_b32(rx, xl + 16, 0, 0);
    const unsigned ep = __builtin_amdgcn_raw_buffer_load_b32(rws, wave ? OFF : 0, WS_EPOCH, AUX_SC1);
    if (tid == 0) ctl[2] = 0;
    BARRIER();
    const v4u msk = pos_mask(c7);
    f4 acc[3];
    acc[0] = f4{0.f, 0.f, 0.f, 0.f};
#pragma unroll
    for (int j = 0; j < 10; ++j) {
      const unsigned xw = j < 8 ? xa[j >> 1] : xb;
      acc[0] = mfma(msk & __builtin_amdgcn_perm(xw, xw, (j & 1) ? 0x03020302u : 0x01000100u), wr[j], acc[0]);
    }
    asm volatile("" :: "v"(pf));
    if (wave) {
      *(LDSP f4*)(P + (wave - 1) * 256 + lane * 4) = acc[0];
      asm volatile("s_waitcnt lgkmcnt(0)" ::: "memory");
      if (lane == 0) __hip_atomic_fetch_add(ctl + 2, 1u, __ATOMIC_RELAXED, __HIP_MEMORY_SCOPE_WORKGROUP);
      if (wave == 2 || b != 0) __builtin_amdgcn_endpgm();
      BARRIER();
      const unsigned tag = __builtin_amdgcn_readfirstlane(ctl[0]);
      BARRIER(); BARRIER();
      const v4u ml = __builtin_amdgcn_raw_buffer_load_b128(rws, (lane & 31) * 1024, WS_ML, AUX_SC1);
      BARRIER(); BARRIER();
      const bool all = none(miss(ml, tag));
      if (!all) {
        BARRIER();
        POLL {
          if (none(miss(__builtin_amdgcn_raw_buffer_load_b128(rws, (lane & 31) * 1024, WS_ML, AUX_SC1), tag))) break;
          AGAIN();
        }
      }
      __builtin_amdgcn_raw_buffer_store_b32(tag, rws, lane ? OFF : 0, WS_EPOCH, AUX_SC1);
      if (all) BARRIER();
      __builtin_amdgcn_endpgm();
    }
    while (__hip_atomic_load(ctl + 2, __ATOMIC_RELAXED, __HIP_MEMORY_SCOPE_WORKGROUP) != 2u) __builtin_amdgcn_s_sleep(1);
    AGAIN();
    acc[1] = *(const LDSP f4*)(P + lane * 4); acc[2] = *(const LDSP f4*)(P + 256 + lane * 4);
    const unsigned tag = tag_of(ep);
    int gqs = WS_GQ + h * GQ_STRIDE;
    PINS(gqs);
    const int rec = s + (s > 2) + (s > 5);
    const int to = (lane & 8) ? OFF : (lane >> 4) * 128 + c7 * 16;
    float q0, q1;
    if (s == 2 || s == 5) {
      fold_tile(s == 2 ? acc[2] : acc[1] + acc[2], q0, q1);
      __builtin_amdgcn_raw_buffer_store_b128(v4u{asu(q0), tag, asu(q1), tag}, rws, to, gqs + (rec + 1) * 512, AUX_SC1);
      fold_tile(s == 2 ? acc[0] + acc[1] : acc[0], q0, q1);
    } else fold_tile((acc[0] + acc[1]) + acc[2], q0, q1);
    __builtin_amdgcn_raw_buffer_store_b128(v4u{asu(q0), tag, asu(q1), tag}, rws, to, gqs + rec * 512, AUX_SC1);
    SB();
    __builtin_amdgcn_s_sleep(QDLY);
    while ((unsigned)__builtin_amdgcn_s_memrealtime() - start < QT) __builtin_amdgcn_s_sleep(2);
    POLL {
#pragma unroll
      for (int i = 0; i < 5; ++i) g[i] = __builtin_amdgcn_raw_buffer_load_b128(rws, lane * 16, gqs + i * 1024, AUX_SC1);
      if (none(miss(g[0], tag) | miss(g[1], tag) | miss(g[2], tag) | miss(g[3], tag) | miss(g[4], tag))) break;
      AGAIN();
    }
    {
      const bool hi = lane >= 32;
      float qa[3][2];
#pragma unroll
      for (int e = 0; e < 2; ++e) {
        qa[0][e] = asf(g[0][2 * e]) + (hi ? 0.f : asf(g[1][2 * e]));
        qa[1][e] = asf(g[2][2 * e]) + (hi ? asf(g[1][2 * e]) : asf(g[3][2 * e]));
        qa[2][e] = asf(g[4][2 * e]) + (hi ? asf(g[3][2 * e]) : 0.f);
      }
#pragma unroll
      for (int t = 0; t < 3; ++t) {
        float a0 = qa[t][0], a1 = qa[t][1], b0 = a0, b1 = a1;
        swap32(a0, b0); swap32(a1, b1);
        qs[32 * t + 4 * c7 + 2 * (r8 & 1) + ((r8 >> 1) & 1)] = pk_bf16(a0 + b0, a1 + b1);
      }
    }
    if (lane == 0) ctl[0] = tag;
    BARRIER();
    BARRIER();
    const unsigned sides = __builtin_amdgcn_readfirstlane(ctl[3]);
    int g1o = (r8 >> 2) * 16384 + (r8 & 3) * G1_STRIDE + c7 * 16, g1q = WS_G1 + (h & 15) * 32768 + SIDE(b & 7) + 3 * (s & 3) * 128;
    int mls = WS_ML + h * 1024;
    PINS(mls); PINS(g1q); PINV(g1o);
    const float mw = ms[c7], M = max8(mw);
    const float L = sum8(ls[c7] * __builtin_amdgcn_exp2f(mw - M));
    __builtin_amdgcn_raw_buffer_store_b128(v4u{asu(M), tag, asu(L), tag}, rws, lane < 8 ? lane * 16 : OFF, mls + s * 128, AUX_SC1);
    SB();
#pragma unroll
    for (int i = 0; i < 3; ++i) g[i] = __builtin_amdgcn_raw_buffer_load_b128(rws, g1o + i * 128, g1q, AUX_SC1);
    SB();
    __builtin_amdgcn_s_sleep(MDLY);
    asm volatile("" :: "v"(g[0]), "v"(g[1]), "v"(g[2]));
    POLL {
      g[3] = __builtin_amdgcn_raw_buffer_load_b128(rws, lane * 16, mls, AUX_SC1);
      if (none(miss(g[3], tag))) break;
      AGAIN();
    }
    const float Ms = asf(g[3][0]), Mg = max_hi(Ms);
    const float Lg = sum_hi(__builtin_amdgcn_exp2f(Ms - Mg) * asf(g[3][2]));
    if (lane == 0) ctl[1] = asu(__builtin_amdgcn_exp2f(M - Mg) / Lg);
    LDSP unsigned* ow0 = os + 8 * c7 + (lane >> 4); LDSP unsigned* ow1 = os + 8 * c7 + 4 + (lane >> 5);
    PINV(ow0); PINV(ow1);
    BARRIER();
    BARRIER();
    BARRIER();
    __builtin_amdgcn_s_sleep(H1DLY);
    unsigned cnt = SPIN_CAP;
    asm volatile(
        "1:\n\t"
        "buffer_load_dwordx4 v[64:67], %[go], %[rs], %[gq] offen sc1\n\tbuffer_load_dwordx4 v[68:71], %[go], %[rs], %[gq] offen offset:128 sc1\n\t"
        "buffer_load_dwordx4 v[72:75], %[go], %[rs], %[gq] offen offset:256 sc1\n\t"
        "s_sub_u32 %[cnt], %[cnt], 1\n\t"
        "s_waitcnt vmcnt(2)\n\tv_bitop3_b32 v65, v65, v67, %[tg] bitop3:0x7e\n\t"
        "s_waitcnt vmcnt(1)\n\tv_bitop3_b32 v69, v69, v71, %[tg] bitop3:0x7e\n\t"
        "v_permlane32_swap_b32 v64, v68\n\tv_permlane32_swap_b32 v66, v70\n\tv_add_f32 v64, v64, v68\n\tv_add_f32 v66, v66, v70\n\t"
        "s_waitcnt vmcnt(0)\n\tv_permlane32_swap_b32 v72, v74\n\tv_bitop3_b32 v73, v73, v75, %[tg] bitop3:0x7e\n\t"
        "v_add_f32 v76, v72, v74\n\tv_add_f32 v77, v72, v74\n\tv_or3_b32 v65, v65, v69, v73\n\t"
        "v_permlane16_swap_b32 v64, v66\n\tv_cmp_ne_u32 vcc, 0, v65\n\tv_permlane16_swap_b32 v76, v77\n\t"
        "v_add_f32 v64, v64, v66\n\tv_add_f32 v76, v76, v77\n\t"
        "s_cbranch_scc1 2f\n\ts_cbranch_vccnz 1b\n"
        "2:\n\ts_nop 0\n\t"
        "v_add_f32_dpp v64, v64, v64 row_ror:8 row_mask:0xf bank_mask:0xf\n\tv_add_f32_dpp v76, v76, v76 row_ror:8 row_mask:0xf bank_mask:0xf\n\t"
        "v_cvt_pk_bf16_f32 v64, v64, v64\n\tv_cvt_pk_bf16_f32 v76, v76, v76\n\t"
        "ds_write_b32 %[o0], v64\n\tds_write_b32 %[o1], v76"
        : [cnt] "+s"(cnt)
        : [go] "v"(g1o), [rs] "s"(rws), [gq] "s"(g1q), [tg] "s"(tag), [o0] "v"(ow0), [o1] "v"(ow1)
        : "memory", "vcc", "scc", V64_77);
    BARRIER();
    __builtin_amdgcn_endpgm();
  }

  {
    const int w = wave - 8;
    unsigned kvo = (unsigned)(h * SK + (SK - W) + (w * 8 + ((s + w) & 7)) * 8) * (DH * 2);
    int lo = r8 * (DH * 2) + c7 * 16;
    PINV(lo); PINS(kvo);
    ARG3(kc, 1, vc, 2, ws0, 6);
    const rsrc_t rk = mk_rsrc((const char*)kc + kvo, (7 * 512 + 8) * DH * 2), rv = mk_rsrc((const char*)vc + kvo, (7 * 512 + 8) * DH * 2);
    v4u kv[48];
    unsigned start = (unsigned)__builtin_amdgcn_s_memrealtime();
#define ISSUE(n) kv[n] = __builtin_amdgcn_raw_buffer_load_b128((n) < 24 ? rk : rv, lo + ((n) % 3) * 128, (((n) % 24) / 3) * 512 * DH * 2, AUX_NT)
    int late = 0;
#define STEP(n) do { SB(); \
      if ((n) <= N_AD) asm volatile("s_waitcnt vmcnt(%0)" :: "n"(WIN - 1) : "memory"); \
      else if ((n) <= N_AH) asm volatile("s_cmp_gt_i32 %0, 0\n\ts_waitcnt vmcnt(%1)\n\ts_cbranch_scc1 1f\n\ts_waitcnt vmcnt(%2)\n1:" \
                                         :: "s"(late), "n"(WIDE - 1), "n"(WIN - 1) : "memory", "scc"); \
      else asm volatile("s_cmp_gt_i32 %0, 0\n\ts_waitcnt vmcnt(%1)\n\ts_cbranch_scc1 1f\n\ts_cmpk_gt_i32 %0, %4\n\ts_waitcnt vmcnt(%2)\n\ts_cbranch_scc1 1f\n\ts_waitcnt vmcnt(%3)\n1:" \
                        :: "s"(late), "n"(WIDE - 1), "n"(WIN - 1), "n"(WIN - 2), "n"(-AHEAD & 0xffff) : "memory", "scc"); \
      ISSUE(n); SB(); \
      if ((n) >= N_AD && (n) % 3 == 2 && (n) < 47) { late = (int)((unsigned)__builtin_amdgcn_s_memrealtime() - start) - (T0 + (n) * DT / 10); PINS(late); SB(); } } while (0)
#pragma unroll
    for (int n = 0; n < WIN; ++n) ISSUE(n);
    BARRIER();
#pragma unroll
    for (int n = WIN; n <= N_QB; ++n) STEP(n);
    BARRIER();
    v4u qv[3];
#pragma unroll
    for (int t = 0; t < 3; ++t) qv[t] = *(const LDSP v4u*)(qs + 32 * t + 4 * c7);
    const unsigned tag = ctl[0];
    constexpr float C2 = 0.07216878364870322f * 1.4426950408889634f;
    constexpr int N_SC = (N_QB + 7 > 23 + WIDE) ? N_QB + 7 : 23 + WIDE;
    float sc[NG];
#pragma unroll
    for (int n = N_QB; n <= N_SC; ++n) {
      if (n > N_QB) STEP(n);
#pragma unroll
      for (int j = 0; j < NG; ++j) {
        if (n == ((N_QB + j > 3 * j + 2 + WIDE) ? N_QB + j : 3 * j + 2 + WIDE)) {
          float a = 0.f;
#pragma unroll
          for (int t = 0; t < 3; ++t)
#pragma unroll
            for (int k = 0; k < 4; ++k) a = dot2(kv[3 * j + t][k], qv[t][k], a);
          sc[j] = sum8(a) * C2;
          PINV(sc[j]);
        }
      }
    }
    float m = sc[0];
#pragma unroll
    for (int j = 1; j < NG; ++j) m = fmaxf(m, sc[j]);
    m = max_hi(m);
    STEP(N_SC + 1);
    float l = 0.f;
#pragma unroll
    for (int j = 0; j < NG; ++j) { sc[j] = __builtin_amdgcn_exp2f(sc[j] - m); l += sc[j]; }
    l = sum_hi(l);
    if (lane == 0) { ms[w] = m; ls[w] = l; }
    STEP(N_SC + 2);
    BARRIER();
    start = __builtin_amdgcn_readfirstlane(ctl[4]);
    const float wt = __builtin_amdgcn_exp2f(m - max8(ms[c7]));
    const v4u msk = pos_mask(c7);
    f4 acc[3] = {{0.f, 0.f, 0.f, 0.f}, {0.f, 0.f, 0.f, 0.f}, {0.f, 0.f, 0.f, 0.f}};
    v4u pa;
#define PV(n) do { if ((n) % 3 == 0) { const float p = sc[((n) - 24) / 3] * wt; pa = msk & pk_bf16(p, p); } acc[(n) % 3] = mfma(pa, kv[n], acc[(n) % 3]); } while (0)
#pragma unroll
    for (int n = 24; n <= N_SC + 2 - WIDE; ++n) PV(n);
#pragma unroll
    for (int n = N_SC + 3; n < 48; ++n) { STEP(n); if (n == 47) BARRIER(); PV(n - WIDE); }
    LDSP float* pp = P + (2 * w + (lane >> 5)) * PSTR + 8 * c7 + 4 * ((lane >> 4) & 1);
    const bool pvalid = (lane >> 5) == ((lane >> 3) & 1);
    const LDSP float* rp = P + 2 * r8 * PSTR + 32 * w + 4 * c7;
    const rsrc_t rws = mk_rsrc((void*)up2m(ws0), WS_END);
    const unsigned sides = __builtin_amdgcn_readfirstlane(ctl[3]);
    int so = (lane & 8) ? OFF : (lane >> 4) * 64 + c7 * 8;
    int ss = WS_G1 + (h & 15) * 32768 + SIDE(b & 7) + (s >> 2) * 16384 + (s & 3) * G1_STRIDE + w * 256;
    float f = asf(ctl[1]);
    v2u sv; sv[1] = tag;
    PINV(so); PINS(ss); PINV(f); PINV(pp); PINV(rp); PINV(sv);
#pragma unroll
    for (int n = 48 - WIDE; n < 45; ++n) PV(n);
#pragma unroll
    for (int n = 45; n < 48; ++n) { PV(n); if (pvalid) *(LDSP f4*)(pp + 64 * (n - 45)) = acc[n - 45]; SB(); }
    BARRIER();
    if (w < 6) {
      const f4 t = *(const LDSP f4*)rp + *(const LDSP f4*)(rp + PSTR);
      float t0 = t[0], t1 = t[1], t2 = t[2], t3 = t[3];
      swap32(t0, t2); t0 += t2;
      swap32(t1, t3); t1 += t3;
      swap16(t0, t1); t0 += t1;
      t0 += dpp<ROR8>(t0);
      sv[0] = asu(t0 * f);
      __builtin_amdgcn_raw_buffer_store_b64(sv, rws, so, ss, AUX_SC1);
    }
    BARRIER();
    __builtin_amdgcn_endpgm();
  }
}

extern "C" void kernel_launch(void* const* d_in, const int* in_sizes, int n_in, void* d_out, int out_size, void* d_ws, size_t ws_size, hipStream_t stream) {
  fused_decode<<<256, NW * 64, 0, stream>>>((const u16*)d_in[0], (const u16*)d_in[1], (const u16*)d_in[2], (const u16*)d_in[3], (const u16*)d_in[4],
                                            (u16*)d_out, (unsigned char*)d_ws, d_ws);
}
```

```cpp
#include <hip/hip_runtime.h>
#include <hip/hip_bf16.h>
#include <stdint.h>

typedef unsigned short u16;
typedef unsigned long long u64;
typedef unsigned v4u __attribute__((ext_vector_type(4)));
typedef unsigned v2u __attribute__((ext_vector_type(2)));
typedef float f4 __attribute__((ext_vector_type(4)));
typedef __bf16 bf16x2 __attribute__((ext_vector_type(2)));
typedef __bf16 bf16x8 __attribute__((ext_vector_type(8)));
typedef __amdgpu_buffer_rsrc_t rsrc_t;
#define LDSP __attribute__((address_space(3)))

constexpr int D = 640, H = 32, SK = 32768, DH = 192, W = 4096;
constexpr int NW = 16, NG = 8;
constexpr int WIN = 3;
constexpr int WIDE = 5;
constexpr int T0 = 110, DT = 292, N_AD = 8;
constexpr int AHEAD = 72, N_AH = 32;
constexpr int N_QB = 20;
constexpr int QDLY = 12, MDLY = 70, H1DLY = 8, H2DLY = 13;
constexpr unsigned QT = 500;
constexpr int CODE_BYTES = 10016;
constexpr unsigned SPIN_CAP = 1u << 20;
constexpr int AUX_NT = 2, AUX_SC1 = 16;
constexpr int OFF = 0x7ffffff0;

constexpr int WS_EPOCH = 0;
constexpr int WS_SD = 1024;
constexpr int WS_GQ = 4096;
constexpr int GQ_STRIDE = 10 * 512;
constexpr int WS_ML = WS_GQ + 32 * GQ_STRIDE;
constexpr int WS_PR = 224 << 10;
constexpr int WS_G1 = 256 << 10;
constexpr int G1_STRIDE = 13 * 128;
constexpr int WS_G2 = 768 << 10;
constexpr int WS_END = WS_G2 + 20 * 32768;
constexpr int PSTR = 208;

__device__ __forceinline__ rsrc_t mk_rsrc(const void* p, int bytes) { return __builtin_amdgcn_make_buffer_rsrc((void*)p, 0, bytes, 0x00020000); }
__device__ __forceinline__ u64 up2m(u64 p) { return (p + 0x1fffff) & ~0x1fffffull; }
__device__ __forceinline__ float asf(unsigned u) { return __builtin_bit_cast(float, u); }
__device__ __forceinline__ unsigned asu(float f) { return __builtin_bit_cast(unsigned, f); }
template <int CTRL> __device__ __forceinline__ float dpp(float x) { return asf(__builtin_amdgcn_mov_dpp(asu(x), CTRL, 0xf, 0xf, true)); }
constexpr int XOR1 = 0xB1, XOR2 = 0x4E, HMIR = 0x141, ROR8 = 0x128;
__device__ __forceinline__ unsigned pk_bf16(float lo, float hi) { bf16x2 r = {(__bf16)lo, (__bf16)hi}; return __builtin_bit_cast(unsigned, r); }
__device__ __forceinline__ float dot2(unsigned a, unsigned b, float c) { return __builtin_amdgcn_fdot2_f32_bf16(__builtin_bit_cast(bf16x2, a), __builtin_bit_cast(bf16x2, b), c, false); }
__device__ __forceinline__ f4 mfma(v4u a, v4u b, f4 c) { return __builtin_amdgcn_mfma_f32_16x16x32_bf16(__builtin_bit_cast(bf16x8, a), __builtin_bit_cast(bf16x8, b), c, 0, 0, 0); }
__device__ __forceinline__ float sum8(float x) { x += dpp<XOR1>(x); x += dpp<XOR2>(x); x += dpp<HMIR>(x); return x; }
__device__ __forceinline__ float max8(float x) { x = fmaxf(x, dpp<XOR1>(x)); x = fmaxf(x, dpp<XOR2>(x)); x = fmaxf(x, dpp<HMIR>(x)); return x; }
__device__ __forceinline__ void swap32(float& a, float& b) { auto r = __builtin_amdgcn_permlane32_swap(asu(a), asu(b), false, false); a = asf(r[0]); b = asf(r[1]); }
__device__ __forceinline__ void swap16(float& a, float& b) { auto r = __builtin_amdgcn_permlane16_swap(asu(a), asu(b), false, false); a = asf(r[0]); b = asf(r[1]); }
__device__ __forceinline__ float sum_hi(float x) { x += dpp<ROR8>(x); float y = x; swap16(x, y); x += y; y = x; swap32(x, y); return x + y; }
__device__ __forceinline__ float max_hi(float x) { x = fmaxf(x, dpp<ROR8>(x)); float y = x; swap16(x, y); x = fmaxf(x, y); y = x; swap32(x, y); return fmaxf(x, y); }
__device__ __forceinline__ unsigned miss(v4u g, unsigned tag) { return (g[1] ^ tag) | (g[3] ^ tag); }
__device__ __forceinline__ bool none(unsigned x) { return __builtin_amdgcn_ballot_w64(x != 0) == 0; }
__device__ __forceinline__ unsigned tag_of(unsigned epoch) { return __builtin_amdgcn_readfirstlane(epoch) * 0x9E3779B1u + 0x7F4A7C15u; }
__device__ __forceinline__ v4u pos_mask(int c7) {
  v4u m;
#pragma unroll
  for (int d = 0; d < 4; ++d) m[d] = ((c7 >> 1) == d) ? ((c7 & 1) ? 0xffff0000u : 0x0000ffffu) : 0u;
  return m;
}
__device__ __forceinline__ void fold_tile(f4 d, float& o0, float& o1) {
  float a = d[0], b = d[2], c = d[1], e = d[3];
  swap32(a, b); swap32(c, e);
  o0 = a + dpp<ROR8>(b); o1 = c + dpp<ROR8>(e);
}

#define SB() __builtin_amdgcn_sched_barrier(0)
#define BARRIER() do { SB(); asm volatile("s_waitcnt lgkmcnt(0)\n\ts_barrier" ::: "memory"); SB(); } while (0)
#define PINV(x) asm volatile("" : "+v"(x))
#define PINS(x) asm volatile("" : "+s"(x))
#define POLL _Pragma("nounroll") for (unsigned spin = 0; spin < SPIN_CAP; ++spin)
#define AGAIN() asm volatile("" ::: "memory")
#define V64_77 "v64", "v65", "v66", "v67", "v68", "v69", "v70", "v71", "v72", "v73", "v74", "v75", "v76", "v77"
#define V78_83 "v78", "v79", "v80", "v81", "v82", "v83"
#define V84_103 "v84", "v85", "v86", "v87", "v88", "v89", "v90", "v91", "v92", "v93", "v94", "v95", "v96", "v97", "v98", "v99", "v100", "v101", "v102", "v103"

__global__ void __launch_bounds__(NW * 64, 4) fused_decode(const u16*, const u16*, const u16*, const u16*, const u16*, u16*, unsigned char*, const void*) {
  __shared__ __attribute__((aligned(16))) float lds[16 * PSTR + 96 + 64 + 8 + 8 + 8];
  LDSP float* P = (LDSP float*)lds;
  LDSP unsigned* qs = (LDSP unsigned*)(P + 16 * PSTR);
  LDSP unsigned* os = qs + 96;
  LDSP float* ms = P + 16 * PSTR + 96 + 64;
  LDSP float* ls = ms + 8;
  LDSP unsigned* ctl = (LDSP unsigned*)(ls + 8);
  asm volatile("s_load_dwordx16 s[64:79], %0, 0x0" :: "s"(__builtin_amdgcn_kernarg_segment_ptr())
               : "s64", "s65", "s66", "s67", "s68", "s69", "s70", "s71", "s72", "s73", "s74", "s75", "s76", "s77", "s78", "s79");
#define ARG3(p0, i0, p1, i1, p2, i2) u64 p0, p1, p2; \
  asm volatile("s_waitcnt lgkmcnt(0)\n\ts_mov_b64 %0, s[%c3:%c4]\n\ts_mov_b64 %1, s[%c5:%c6]\n\ts_mov_b64 %2, s[%c7:%c8]" : "=s"(p0), "=s"(p1), "=s"(p2) \
               : "i"(64 + 2 * i0), "i"(65 + 2 * i0), "i"(64 + 2 * i1), "i"(65 + 2 * i1), "i"(64 + 2 * i2), "i"(65 + 2 * i2))
  unsigned pf = 0;
  if (threadIdx.x < 128) {
    u64 pc; asm volatile("s_getpc_b64 %0" : "=s"(pc));
    pf = __builtin_amdgcn_raw_buffer_load_b32(mk_rsrc((void*)(pc & ~127ull), CODE_BYTES), threadIdx.x * 128 + 128, 0, 0);
  }
  SB();
  const int b = blockIdx.x, jb = b >> 3;
  const int h = (b & 7) * 4 + (jb >> 3), s = jb & 7;
  const int tid = threadIdx.x, wave = __builtin_amdgcn_readfirstlane(tid >> 6), lane = tid & 63, r8 = lane >> 3, c7 = lane & 7;
#define SIDE(c) (((sides >> (c)) & 1) << 13)

  if (wave >= 3 && wave < 8) {
    const int rq = s & 3, ch = s >> 2;
    ARG3(wo, 4, out, 5, ws0, 6);
    const u64 ws = up2m(ws0);
    const rsrc_t rws = mk_rsrc((void*)ws, WS_END), rout = mk_rsrc((void*)out, D * 2);
    unsigned ep = 0;
    const unsigned start = (unsigned)__builtin_amdgcn_s_memrealtime() - 15;
    if (b < 8) {
      __builtin_amdgcn_raw_buffer_store_b128(v4u{0u, 0u, 0u, 0u}, rws, lane < 16 ? r8 * 8192 + c7 * 16 : OFF, WS_PR + b * 1024 + wave * 128, AUX_SC1);
      ep = __builtin_amdgcn_raw_buffer_load_b32(rws, 0, WS_EPOCH, AUX_SC1);
    }
    BARRIER();
    const rsrc_t ro = mk_rsrc((const u16*)wo + (size_t)(h * DH) * D + 320 * ch + 64 * (wave - 3), DH * D * 2);
    v4u msk = pos_mask(c7);
    int wl = (8 * (r8 & 3) + (r8 >> 2)) * (D * 2) + c7 * 16;
    int wsf[3];
#pragma unroll
    for (int i = 0; i < 3; ++i) { const int n = 3 * rq + i; wsf[i] = (32 * (n >> 1) + 2 * (n & 1)) * (D * 2); PINS(wsf[i]); }
    const int yl = 4 * (wave - 3) + (lane >> 4), yp = h * 4 + rq;
    int ys = WS_G2 + (yp >> 6) * 16384 + (yp & 63) * 128;
    const int lc = 4 * ((jb < 16 ? b & 3 : 4) + 5 * ((b >> 2) & 1)) + ((jb >> 2) & 3), qd = jb & 3;
    const bool fin = wave == 4 && (jb < 16 || (b & 3) == 0);
    int fo = (lane >> 1) * 128 + (lane & 1) * 16;
    int oo = (lane & 30) ? OFF : 2 * (64 * (lc >> 2) + 8 * (2 * qd + (lane & 1)) + 4 * (lc & 1) + (lc & 2) + (lane >> 5));
    const LDSP unsigned* op = os + 8 * r8;
    PINV(fo); PINV(oo); PINV(wl); PINV(msk); PINV(op); PINS(ys);
    if (b < 8) {
      asm volatile("s_waitcnt vmcnt(0)" ::: "memory");
      const int first = (wave ^ b) & 1, o0 = WS_PR + b * 1024 + wave * 128 + first * 8192, o1 = o0 ^ 8192;
      u64 c0, c1, c2; unsigned unused;
      asm volatile("s_memtime %1\n\ts_waitcnt lgkmcnt(0)\n\ts_load_dword %0, %4, %5\n\ts_waitcnt lgkmcnt(0)\n\ts_memtime %2\n\ts_waitcnt lgkmcnt(0)\n\t"
                   "s_load_dword %0, %4, %6\n\ts_waitcnt lgkmcnt(0)\n\ts_memtime %3\n\ts_waitcnt lgkmcnt(0)"
                   : "=&s"(unused), "=&s"(c0), "=&s"(c1), "=&s"(c2) : "s"(ws), "s"(o0), "s"(o1) : "memory");
      const int hi = (__builtin_amdgcn_s_getreg(20 | (3 << 11)) >> 2) & 1;
      int d = max(-1000, min(1000, (int)(c1 - c0) - (int)(c2 - c1)));
      if (first ^ hi) d = -d;
      __builtin_amdgcn_raw_buffer_store_b64(v2u{(unsigned)(2 * d + hi), tag_of(ep)}, rws, lane ? OFF : b * 128 + wave * 8, WS_SD, AUX_SC1);
      if (wave == 3) __builtin_amdgcn_raw_buffer_store_b64(v2u{start, tag_of(ep)}, rws, lane ? OFF : b * 128 + 16, WS_SD, AUX_SC1);
    }
    BARRIER();
    const unsigned tag = __builtin_amdgcn_readfirstlane(ctl[0]);
    if (wave == 7) {
      v2u sd;
      POLL {
        sd = __builtin_amdgcn_raw_buffer_load_b64(rws, r8 * 128 + (c7 < 2 ? 3 : c7) * 8, WS_SD, AUX_SC1);
        if (none(sd[1] ^ tag)) break;
        AGAIN();
      }
      const float dd = sum_hi(sum8(c7 >= 3 ? (float)((int)sd[0] >> 1) : 0.f));
      const u64 bl = __builtin_amdgcn_ballot_w64(((sd[0] & 1) != 0) != (dd > 0.f));
      const unsigned sb = (bl & 1) | ((bl >> 7) & 2) | ((bl >> 14) & 4) | ((bl >> 21) & 8);
      const float ds = sum_hi(sum8(c7 == 2 ? (float)(int)(sd[0] - start) : 0.f)) * 0.125f;
      if (lane == 0) { ctl[3] = sb | ((sb ^ 15u) << 4); ctl[4] = start + (int)ds; }
    }
    BARRIER();
    const unsigned sides = __builtin_amdgcn_readfirstlane(ctl[3]);
    int fs[4];
#pragma unroll
    for (int i = 0; i < 4; ++i) { fs[i] = WS_G2 + (lc % 20) * 32768 + (i >> 1) * 16384 + SIDE(b & 7) + (i & 1) * 4096 + qd * 32; PINS(fs[i]); }
    int yo = (lane & 8) ? OFF : yl * 32768 + SIDE(4 * ch + ((wave - 3) & 3)) + c7 * 16;
    PINV(yo);
    if (fin) {
      v4u g[4];
#pragma unroll
      for (int i = 0; i < 4; ++i) g[i] = __builtin_amdgcn_raw_buffer_load_b128(rws, fo, fs[i], AUX_SC1);
      asm volatile("" :: "v"(g[0]), "v"(g[1]), "v"(g[2]), "v"(g[3]));
    }
    BARRIER();
    v4u wr[6];
#pragma unroll
    for (int j = 0; j < 6; ++j) wr[j] = __builtin_amdgcn_raw_buffer_load_b128(ro, wl, wsf[j >> 1] + (j & 1) * 4 * (D * 2), AUX_NT);
    BARRIER();
    BARRIER();
    asm volatile(
        "v_mov_b32 v101, %[tg]\n\tv_mov_b32 v103, %[tg]\n\t"
        "s_barrier\n\t"
        "ds_read_b128 v[64:67], %[op]\n\tds_read_b64 v[68:69], %[op] offset:16\n\t"
        "s_waitcnt lgkmcnt(1)\n\t"
        "v_and_b32 v72, v64, %[m0]\n\tv_and_b32 v73, v64, %[m1]\n\tv_and_b32 v74, v64, %[m2]\n\tv_and_b32 v75, v64, %[m3]\n\t"
        "v_and_b32 v76, v65, %[m0]\n\tv_and_b32 v77, v65, %[m1]\n\t"
        "v_mfma_f32_16x16x32_bf16 v[96:99], v[72:75], %[w0], 0\n\t"
        "v_and_b32 v78, v65, %[m2]\n\tv_and_b32 v79, v65, %[m3]\n\tv_and_b32 v80, v66, %[m0]\n\tv_and_b32 v81, v66, %[m1]\n\t"
        "v_mfma_f32_16x16x32_bf16 v[96:99], v[76:79], %[w1], v[96:99]\n\t"
        "v_and_b32 v82, v66, %[m2]\n\tv_and_b32 v83, v66, %[m3]\n\tv_and_b32 v84, v67, %[m0]\n\tv_and_b32 v85, v67, %[m1]\n\t"
        "v_mfma_f32_16x16x32_bf16 v[96:99], v[80:83], %[w2], v[96:99]\n\t"
        "v_and_b32 v86, v67, %[m2]\n\tv_and_b32 v87, v67, %[m3]\n\ts_waitcnt lgkmcnt(0)\n\tv_and_b32 v88, v68, %[m0]\n\tv_and_b32 v89, v68, %[m1]\n\t"
        "v_mfma_f32_16x16x32_bf16 v[96:99], v[84:87], %[w3], v[96:99]\n\t"
        "v_and_b32 v90, v68, %[m2]\n\tv_and_b32 v91, v68, %[m3]\n\tv_and_b32 v92, v69, %[m0]\n\tv_and_b32 v93, v69, %[m1]\n\t"
        "v_mfma_f32_16x16x32_bf16 v[96:99], v[88:91], %[w4], v[96:99]\n\t"
        "v_and_b32 v94, v69, %[m2]\n\tv_and_b32 v95, v69, %[m3]\n\ts_nop 1\n\t"
        "v_mfma_f32_16x16x32_bf16 v[96:99], v[92:95], %[w5], v[96:99]\n\t"
        "s_nop 7\n\t"
        "v_permlane32_swap_b32 v96, v98\n\tv_permlane32_swap_b32 v97, v99\n\ts_nop 0\n\t"
        "v_add_f32_dpp v100, v98, v96 row_ror:8 row_mask:0xf bank_mask:0xf\n\tv_add_f32_dpp v102, v99, v97 row_ror:8 row_mask:0xf bank_mask:0xf\n\t"
        "buffer_store_dwordx4 v[100:103], %[yo], %[rs], %[ys] offen sc1\n\ts_nop 1"
        :: [tg] "s"(tag), [op] "v"(op), [m0] "v"(msk[0]), [m1] "v"(msk[1]), [m2] "v"(msk[2]), [m3] "v"(msk[3]),
           [w0] "v"(wr[0]), [w1] "v"(wr[1]), [w2] "v"(wr[2]), [w3] "v"(wr[3]), [w4] "v"(wr[4]), [w5] "v"(wr[5]), [yo] "v"(yo), [rs] "s"(rws), [ys] "s"(ys)
        : "memory", V64_77, V78_83, V84_103);
    if (fin) {
      __builtin_amdgcn_s_sleep(H2DLY);
      unsigned cnt = SPIN_CAP;
      asm volatile(
          "1:\n\ts_nop 2\n\t"
          "buffer_load_dwordx4 v[64:67], %[fo], %[rs], %[f0] offen sc1\n\tbuffer_load_dwordx4 v[68:71], %[fo], %[rs], %[f1] offen sc1\n\t"
          "buffer_load_dwordx4 v[72:75], %[fo], %[rs], %[f2] offen sc1\n\tbuffer_load_dwordx4 v[76:79], %[fo], %[rs], %[f3] offen sc1\n\t"
          "s_sub_u32 %[cnt], %[cnt], 1\n\t"
          "s_waitcnt vmcnt(3)\n\tv_bitop3_b32 v65, v65, v67, %[tg] bitop3:0x7e\n\t"
          "s_waitcnt vmcnt(2)\n\tv_bitop3_b32 v69, v69, v71, %[tg] bitop3:0x7e\n\tv_add_f32 v80, v64, v68\n\tv_add_f32 v81, v66, v70\n\t"
          "s_waitcnt vmcnt(1)\n\tv_bitop3_b32 v73, v73, v75, %[tg] bitop3:0x7e\n\tv_or3_b32 v65, v65, v69, v73\n\t"
          "s_waitcnt vmcnt(0)\n\tv_bitop3_b32 v77, v77, v79, %[tg] bitop3:0x7e\n\tv_add_f32 v72, v72, v76\n\tv_add_f32 v74, v74, v78\n\t"
          "v_or_b32 v65, v65, v77\n\tv_add_f32 v80, v80, v72\n\tv_add_f32 v81, v81, v74\n\t"
          "v_cmpx_ne_u32 vcc, 0, v65\n\t"
          "s_cbranch_scc1 2f\n\ts_cbranch_execnz 1b\n"
          "2:\n\ts_mov_b64 exec, -1\n\ts_nop 1\n\t"
          "v_permlane32_swap_b32 v80, v81\n\tv_add_f32 v82, v80, v81\n\tv_add_f32 v83, v80, v81\n\ts_nop 1\n\t"
          "v_permlane16_swap_b32 v82, v83\n\tv_add_f32 v80, v82, v83\n\ts_nop 1\n\t"
          "v_add_f32_dpp v80, v80, v80 row_ror:8 row_mask:0xf bank_mask:0xf\n\ts_nop 1\n\t"
          "v_add_f32_dpp v80, v80, v80 row_ror:4 row_mask:0xf bank_mask:0xf\n\ts_nop 1\n\t"
          "v_add_f32_dpp v80, v80, v80 quad_perm:[2,3,0,1] row_mask:0xf bank_mask:0xf\n\t"
          "v_cvt_pk_bf16_f32 v80, v80, v80\n\t"
          "buffer_store_short v80, %[oo], %[ro], 0 offen"
          : [cnt] "+s"(cnt)
          : [fo] "v"(fo), [rs] "s"(rws), [f0] "s"(fs[0]), [f1] "s"(fs[1]), [f2] "s"(fs[2]), [f3] "s"(fs[3]), [tg] "s"(tag), [oo] "v"(oo), [ro] "s"(rout)
          : "memory", "vcc", "scc", V64_77, V78_83);
    }
    __builtin_amdgcn_endpgm();
  }

  if (wave < 3) {
    const int u = 3 * s + wave;
    int wl = r8 * 10 * (H * DH * 2) + c7 * 16, xl = 20 * r8 + (u & 7) * 160;
    unsigned us = (u & 7) * 80 * (H * DH * 2) + (u >> 3) * 128 + h * (DH * 2);
    PINV(wl); PINV(xl); PINS(us);
    ARG3(wq, 3, x, 0, ws0, 6);
    const rsrc_t rq = mk_rsrc((const char*)wq + us, 79 * H * DH * 2 + 128), rx = mk_rsrc((void*)x, D * 2), rws = mk_rsrc((void*)up2m(ws0), WS_END);
    v4u wr[10], g[5];
    const unsigned start = (unsigned)__builtin_amdgcn_s_memrealtime();
#pragma unroll
    for (int j = 0; j < 10; ++j) wr[j] = __builtin_amdgcn_raw_buffer_load_b128(rq, wl, j * (H * DH * 2), AUX_NT);
    const v4u xa = __builtin_amdgcn_raw_buffer_load_b128(rx, xl, 0, 0);
    const unsigned xb = __builtin_amdgcn_raw_buffer_load_b32(rx, xl + 16, 0, 0);
    const unsigned ep = __builtin_amdgcn_raw_buffer_load_b32(rws, wave ? OFF : 0, WS_EPOCH, AUX_SC1);
    if (tid == 0) ctl[2] = 0;
    BARRIER();
    const v4u msk = pos_mask(c7);
    f4 acc[3];
    acc[0] = f4{0.f, 0.f, 0.f, 0.f};
#pragma unroll
    for (int j = 0; j < 10; ++j) {
      const unsigned xw = j < 8 ? xa[j >> 1] : xb;
      acc[0] = mfma(msk & __builtin_amdgcn_perm(xw, xw, (j & 1) ? 0x03020302u : 0x01000100u), wr[j], acc[0]);
    }
    asm volatile("" :: "v"(pf));
    if (wave) {
      *(LDSP f4*)(P + (wave - 1) * 256 + lane * 4) = acc[0];
      asm volatile("s_waitcnt lgkmcnt(0)" ::: "memory");
      if (lane == 0) __hip_atomic_fetch_add(ctl + 2, 1u, __ATOMIC_RELAXED, __HIP_MEMORY_SCOPE_WORKGROUP);
      if (wave == 2 || b != 0) __builtin_amdgcn_endpgm();
      BARRIER();
      const unsigned tag = __builtin_amdgcn_readfirstlane(ctl[0]);
      BARRIER(); BARRIER();
      const v4u ml = __builtin_amdgcn_raw_buffer_load_b128(rws, (lane & 31) * 1024, WS_ML, AUX_SC1);
      BARRIER(); BARRIER();
      const bool all = none(miss(ml, tag));
      if (!all) {
        BARRIER();
        POLL {
          if (none(miss(__builtin_amdgcn_raw_buffer_load_b128(rws, (lane & 31) * 1024, WS_ML, AUX_SC1), tag))) break;
          AGAIN();
        }
      }
      __builtin_amdgcn_raw_buffer_store_b32(tag, rws, lane ? OFF : 0, WS_EPOCH, AUX_SC1);
      if (all) BARRIER();
      __builtin_amdgcn_endpgm();
    }
    while (__hip_atomic_load(ctl + 2, __ATOMIC_RELAXED, __HIP_MEMORY_SCOPE_WORKGROUP) != 2u) __builtin_amdgcn_s_sleep(1);
    AGAIN();
    acc[1] = *(const LDSP f4*)(P + lane * 4); acc[2] = *(const LDSP f4*)(P + 256 + lane * 4);
    const unsigned tag = tag_of(ep);
    int gqs = WS_GQ + h * GQ_STRIDE;
    PINS(gqs);
    const int rec = s + (s > 2) + (s > 5);
    const int to = (lane & 8) ? OFF : (lane >> 4) * 128 + c7 * 16;
    float q0, q1;
    if (s == 2 || s == 5) {
      fold_tile(s == 2 ? acc[2] : acc[1] + acc[2], q0, q1);
      __builtin_amdgcn_raw_buffer_store_b128(v4u{asu(q0), tag, asu(q1), tag}, rws, to, gqs + (rec + 1) * 512, AUX_SC1);
      fold_tile(s == 2 ? acc[0] + acc[1] : acc[0], q0, q1);
    } else fold_tile((acc[0] + acc[1]) + acc[2], q0, q1);
    __builtin_amdgcn_raw_buffer_store_b128(v4u{asu(q0), tag, asu(q1), tag}, rws, to, gqs + rec * 512, AUX_SC1);
    SB();
    __builtin_amdgcn_s_sleep(QDLY);
    while ((unsigned)__builtin_amdgcn_s_memrealtime() - start < QT) __builtin_amdgcn_s_sleep(2);
    POLL {
#pragma unroll
      for (int i = 0; i < 5; ++i) g[i] = __builtin_amdgcn_raw_buffer_load_b128(rws, lane * 16, gqs + i * 1024, AUX_SC1);
      if (none(miss(g[0], tag) | miss(g[1], tag) | miss(g[2], tag) | miss(g[3], tag) | miss(g[4], tag))) break;
      AGAIN();
    }
    {
      const bool hi = lane >= 32;
      float qa[3][2];
#pragma unroll
      for (int e = 0; e < 2; ++e) {
        qa[0][e] = asf(g[0][2 * e]) + (hi ? 0.f : asf(g[1][2 * e]));
        qa[1][e] = asf(g[2][2 * e]) + (hi ? asf(g[1][2 * e]) : asf(g[3][2 * e]));
        qa[2][e] = asf(g[4][2 * e]) + (hi ? asf(g[3][2 * e]) : 0.f);
      }
#pragma unroll
      for (int t = 0; t < 3; ++t) {
        float a0 = qa[t][0], a1 = qa[t][1], b0 = a0, b1 = a1;
        swap32(a0, b0); swap32(a1, b1);
        qs[32 * t + 4 * c7 + 2 * (r8 & 1) + ((r8 >> 1) & 1)] = pk_bf16(a0 + b0, a1 + b1);
      }
    }
    if (lane == 0) ctl[0] = tag;
    BARRIER();
    BARRIER();
    const unsigned sides = __builtin_amdgcn_readfirstlane(ctl[3]);
    int g1o = (r8 >> 2) * 16384 + (r8 & 3) * G1_STRIDE + c7 * 16, g1q = WS_G1 + (h & 15) * 32768 + SIDE(b & 7) + 3 * (s & 3) * 128;
    int mls = WS_ML + h * 1024;
    PINS(mls); PINS(g1q); PINV(g1o);
    const float mw = ms[c7], M = max8(mw);
    const float L = sum8(ls[c7] * __builtin_amdgcn_exp2f(mw - M));
    __builtin_amdgcn_raw_buffer_store_b128(v4u{asu(M), tag, asu(L), tag}, rws, lane < 8 ? lane * 16 : OFF, mls + s * 128, AUX_SC1);
    SB();
#pragma unroll
    for (int i = 0; i < 3; ++i) g[i] = __builtin_amdgcn_raw_buffer_load_b128(rws, g1o + i * 128, g1q, AUX_SC1);
    SB();
    __builtin_amdgcn_s_sleep(MDLY);
    asm volatile("" :: "v"(g[0]), "v"(g[1]), "v"(g[2]));
    POLL {
      g[3] = __builtin_amdgcn_raw_buffer_load_b128(rws, lane * 16, mls, AUX_SC1);
      if (none(miss(g[3], tag))) break;
      AGAIN();
    }
    const float Ms = asf(g[3][0]), Mg = max_hi(Ms);
    const float Lg = sum_hi(__builtin_amdgcn_exp2f(Ms - Mg) * asf(g[3][2]));
    if (lane == 0) ctl[1] = asu(__builtin_amdgcn_exp2f(M - Mg) / Lg);
    LDSP unsigned* ow0 = os + 8 * c7 + (lane >> 4); LDSP unsigned* ow1 = os + 8 * c7 + 4 + (lane >> 5);
    PINV(ow0); PINV(ow1);
    BARRIER();
    BARRIER();
    BARRIER();
    __builtin_amdgcn_s_sleep(H1DLY);
    unsigned cnt = SPIN_CAP;
    asm volatile(
        "1:\n\t"
        "buffer_load_dwordx4 v[64:67], %[go], %[rs], %[gq] offen sc1\n\tbuffer_load_dwordx4 v[68:71], %[go], %[rs], %[gq] offen offset:128 sc1\n\t"
        "buffer_load_dwordx4 v[72:75], %[go], %[rs], %[gq] offen offset:256 sc1\n\t"
        "s_sub_u32 %[cnt], %[cnt], 1\n\t"
        "s_waitcnt vmcnt(2)\n\tv_bitop3_b32 v65, v65, v67, %[tg] bitop3:0x7e\n\t"
        "s_waitcnt vmcnt(1)\n\tv_bitop3_b32 v69, v69, v71, %[tg] bitop3:0x7e\n\t"
        "v_permlane32_swap_b32 v64, v68\n\tv_permlane32_swap_b32 v66, v70\n\tv_add_f32 v64, v64, v68\n\tv_add_f32 v66, v66, v70\n\t"
        "s_waitcnt vmcnt(0)\n\tv_permlane32_swap_b32 v72, v74\n\tv_bitop3_b32 v73, v73, v75, %[tg] bitop3:0x7e\n\t"
        "v_add_f32 v76, v72, v74\n\tv_add_f32 v77, v72, v74\n\tv_or3_b32 v65, v65, v69, v73\n\t"
        "v_permlane16_swap_b32 v64, v66\n\tv_cmp_ne_u32 vcc, 0, v65\n\tv_permlane16_swap_b32 v76, v77\n\t"
        "v_add_f32 v64, v64, v66\n\tv_add_f32 v76, v76, v77\n\t"
        "s_cbranch_scc1 2f\n\ts_cbranch_vccnz 1b\n"
        "2:\n\ts_nop 0\n\t"
        "v_add_f32_dpp v64, v64, v64 row_ror:8 row_mask:0xf bank_mask:0xf\n\tv_add_f32_dpp v76, v76, v76 row_ror:8 row_mask:0xf bank_mask:0xf\n\t"
        "v_cvt_pk_bf16_f32 v64, v64, v64\n\tv_cvt_pk_bf16_f32 v76, v76, v76\n\t"
        "ds_write_b32 %[o0], v64\n\tds_write_b32 %[o1], v76"
        : [cnt] "+s"(cnt)
        : [go] "v"(g1o), [rs] "s"(rws), [gq] "s"(g1q), [tg] "s"(tag), [o0] "v"(ow0), [o1] "v"(ow1)
        : "memory", "vcc", "scc", V64_77);
    BARRIER();
    __builtin_amdgcn_endpgm();
  }

  {
    const int w = wave - 8;
    unsigned kvo = (unsigned)(h * SK + (SK - W) + (w * 8 + ((s + w) & 7)) * 8) * (DH * 2);
    int lo = r8 * (DH * 2) + c7 * 16;
    PINV(lo); PINS(kvo);
    ARG3(kc, 1, vc, 2, ws0, 6);
    const rsrc_t rk = mk_rsrc((const char*)kc + kvo, (7 * 512 + 8) * DH * 2), rv = mk_rsrc((const char*)vc + kvo, (7 * 512 + 8) * DH * 2);
    v4u kv[48];
    unsigned start = (unsigned)__builtin_amdgcn_s_memrealtime();
#define ISSUE(n) kv[n] = __builtin_amdgcn_raw_buffer_load_b128((n) < 24 ? rk : rv, lo + ((n) % 3) * 128, (((n) % 24) / 3) * 512 * DH * 2, AUX_NT)
    int late = 0;
#define STEP(n) do { SB(); \
      if ((n) <= N_AD) asm volatile("s_waitcnt vmcnt(%0)" :: "n"(WIN - 1) : "memory"); \
      else if ((n) <= N_AH) asm volatile("s_cmp_gt_i32 %0, 0\n\ts_waitcnt vmcnt(%1)\n\ts_cbranch_scc1 1f\n\ts_waitcnt vmcnt(%2)\n1:" \
                                         :: "s"(late), "n"(WIDE - 1), "n"(WIN - 1) : "memory", "scc"); \
      else asm volatile("s_cmp_gt_i32 %0, 0\n\ts_waitcnt vmcnt(%1)\n\ts_cbranch_scc1 1f\n\ts_cmpk_gt_i32 %0, %4\n\ts_waitcnt vmcnt(%2)\n\ts_cbranch_scc1 1f\n\ts_waitcnt vmcnt(%3)\n1:" \
                        :: "s"(late), "n"(WIDE - 1), "n"(WIN - 1), "n"(WIN - 2), "n"(-AHEAD & 0xffff) : "memory", "scc"); \
      ISSUE(n); SB(); \
      if ((n) >= N_AD && (n) % 3 == 2 && (n) < 47) { late = (int)((unsigned)__builtin_amdgcn_s_memrealtime() - start) - (T0 + (n) * DT / 10); PINS(late); SB(); } } while (0)
#pragma unroll
    for (int n = 0; n < WIN; ++n) ISSUE(n);
    BARRIER();
#pragma unroll
    for (int n = WIN; n <= N_QB; ++n) STEP(n);
    BARRIER();
    v4u qv[3];
#pragma unroll
    for (int t = 0; t < 3; ++t) qv[t] = *(const LDSP v4u*)(qs + 32 * t + 4 * c7);
    const unsigned tag = ctl[0];
    constexpr float C2 = 0.07216878364870322f * 1.4426950408889634f;
    constexpr int N_SC = (N_QB + 7 > 23 + WIDE) ? N_QB + 7 : 23 + WIDE;
    float sc[NG];
#pragma unroll
    for (int n = N_QB; n <= N_SC; ++n) {
      if (n > N_QB) STEP(n);
#pragma unroll
      for (int j = 0; j < NG; ++j) {
        if (n == ((N_QB + j > 3 * j + 2 + WIDE) ? N_QB + j : 3 * j + 2 + WIDE)) {
          float a = 0.f;
#pragma unroll
          for (int t = 0; t < 3; ++t)
#pragma unroll
            for (int k = 0; k < 4; ++k) a = dot2(kv[3 * j + t][k], qv[t][k], a);
          sc[j] = sum8(a) * C2;
          PINV(sc[j]);
        }
      }
    }
    float m = sc[0];
#pragma unroll
    for (int j = 1; j < NG; ++j) m = fmaxf(m, sc[j]);
    m = max_hi(m);
    STEP(N_SC + 1);
    float l = 0.f;
#pragma unroll
    for (int j = 0; j < NG; ++j) { sc[j] = __builtin_amdgcn_exp2f(sc[j] - m); l += sc[j]; }
    l = sum_hi(l);
    if (lane == 0) { ms[w] = m; ls[w] = l; }
    STEP(N_SC + 2);
    BARRIER();
    start = __builtin_amdgcn_readfirstlane(ctl[4]);
    const float wt = __builtin_amdgcn_exp2f(m - max8(ms[c7]));
    const v4u msk = pos_mask(c7);
    f4 acc[3] = {{0.f, 0.f, 0.f, 0.f}, {0.f, 0.f, 0.f, 0.f}, {0.f, 0.f, 0.f, 0.f}};
    v4u pa;
#define PV(n) do { if ((n) % 3 == 0) { const float p = sc[((n) - 24) / 3] * wt; pa = msk & pk_bf16(p, p); } acc[(n) % 3] = mfma(pa, kv[n], acc[(n) % 3]); } while (0)
#pragma unroll
    for (int n = 24; n <= N_SC + 2 - WIDE; ++n) PV(n);
#pragma unroll
    for (int n = N_SC + 3; n < 48; ++n) { STEP(n); if (n == 47) BARRIER(); PV(n - WIDE); }
    LDSP float* pp = P + (2 * w + (lane >> 5)) * PSTR + 8 * c7 + 4 * ((lane >> 4) & 1);
    const bool pvalid = (lane >> 5) == ((lane >> 3) & 1);
    const LDSP float* rp = P + 2 * r8 * PSTR + 32 * w + 4 * c7;
    const rsrc_t rws = mk_rsrc((void*)up2m(ws0), WS_END);
    const unsigned sides = __builtin_amdgcn_readfirstlane(ctl[3]);
    int so = (lane & 8) ? OFF : (lane >> 4) * 64 + c7 * 8;
    int ss = WS_G1 + (h & 15) * 32768 + SIDE(b & 7) + (s >> 2) * 16384 + (s & 3) * G1_STRIDE + w * 256;
    float f = asf(ctl[1]);
    v2u sv; sv[1] = tag;
    PINV(so); PINS(ss); PINV(f); PINV(pp); PINV(rp); PINV(sv);
#pragma unroll
    for (int n = 48 - WIDE; n < 45; ++n) PV(n);
#pragma unroll
    for (int n = 45; n < 48; ++n) { PV(n); if (pvalid) *(LDSP f4*)(pp + 64 * (n - 45)) = acc[n - 45]; SB(); }
    BARRIER();
    if (w < 6) {
      const f4 t = *(const LDSP f4*)rp + *(const LDSP f4*)(rp + PSTR);
      float t0 = t[0], t1 = t[1], t2 = t[2], t3 = t[3];
      swap32(t0, t2); t0 += t2;
      swap32(t1, t3); t1 += t3;
      swap16(t0, t1); t0 += t1;
      t0 += dpp<ROR8>(t0);
      sv[0] = asu(t0 * f);
      __builtin_amdgcn_raw_buffer_store_b64(sv, rws, so, ss, AUX_SC1);
    }
    BARRIER();
    __builtin_amdgcn_endpgm();
  }
}

extern "C" void kernel_launch(void* const* d_in, const int* in_sizes, int n_in, void* d_out, int out_size, void* d_ws, size_t ws_size, hipStream_t stream) {
  fused_decode<<<256, NW * 64, 0, stream>>>((const u16*)d_in[0], (const u16*)d_in[1], (const u16*)d_in[2], (const u16*)d_in[3], (const u16*)d_in[4],
                                            (u16*)d_out, (unsigned char*)d_ws, d_ws);
}
```
